```python
import torch

def _softmax(x, axis=-1):
    return torch.softmax(x, dim=axis)
def _gelu(x):
    return torch.nn.functional.gelu(x, approximate="tanh")
def _silu(x):
    return torch.nn.functional.silu(x)
def _sigmoid(x):
    return torch.sigmoid(x)
def _layernorm(x, axis=-1, eps=1e-5):
    m = x.mean(dim=axis, keepdim=True)
    v = x.var(dim=axis, keepdim=True, unbiased=False)
    return (x - m) / torch.sqrt(v + eps)
def _scatter_add(tgt, idx, src, axis=0):
    return tgt.clone().index_add_(axis, idx, src)
def _segment_sum(src, seg, n):
    out = torch.zeros((n,) + src.shape[1:], dtype=src.dtype)
    return out.index_add_(0, seg, src)
def _pad(x, pw):
    flat = tuple(v for pair in reversed(pw) for v in pair)
    return torch.nn.functional.pad(x, flat)

kernel_name = 'recurrent_scan__sliding_local_dilated__dense_activation_gelu__prompt'

def setup_inputs():

    _w = (lambda a: (lambda t: t.to(torch.bfloat16) if t.is_floating_point() else t)(torch.as_tensor(a)))
    def _A_setup():
        B, S, D = 1, 2048, 576
        return {
            "x": _w((lambda *s: torch.randn(*s, dtype=torch.bfloat16) * 0.05)(B, S, D)),
            "wqkv": _w((lambda *s: torch.randn(*s, dtype=torch.bfloat16) * 0.2)(D, 3 * D)),
            "wo": _w((lambda *s: torch.randn(*s, dtype=torch.bfloat16) * 0.2)(D, D)),
        }
    _A_in = _A_setup()

    def _B_setup():
        B, S, D, F = 1, 2048, 576, 4608
        return {
            "x": _w((lambda *s: torch.randn(*s, dtype=torch.bfloat16) * 0.05)(B, S, D)),
            "w1": _w((lambda *s: torch.randn(*s, dtype=torch.bfloat16) * 0.8)(D, F) * (D ** -0.5)),
            "b1": _w((lambda *s: torch.randn(*s, dtype=torch.bfloat16) * 0.8)(F) * 0.01),
            "w2": _w((lambda *s: torch.randn(*s, dtype=torch.bfloat16) * 0.1)(F, D) * (F ** -0.5)),
            "b2": _w((lambda *s: torch.randn(*s, dtype=torch.bfloat16) * 0.1)(D) * 0.01),
        }
    _B_in = _B_setup()

    return {
        "A_x": _A_in["x"],
        "A_wqkv": _A_in["wqkv"],
        "A_wo": _A_in["wo"],
        "B_x": _B_in["x"],
        "B_w1": _B_in["w1"],
        "B_b1": _B_in["b1"],
        "B_w2": _B_in["w2"],
        "B_b2": _B_in["b2"],
    }

def reference(A_x, A_wqkv, A_wo, B_x, B_w1, B_b1, B_w2, B_b2):
    def _A_ref(x, wqkv, wo):
        B, S, D = x.shape
        H = 32; Dh = D // H
        W, dil = 8, 2
        qkv = (lambda x, *d: x.permute(*d))((x @ wqkv).reshape(B, S, 3, H, Dh), 2, 0, 3, 1, 4)
        q, k, v = qkv[0], qkv[1], qkv[2]
        scale = Dh ** -0.5
        pos = torch.arange(S)
        off = torch.arange(W) * dil
        idx = pos[:, None] - off[None, :]
        mask = idx >= 0
        idx_c = torch.where(mask, idx, 0).reshape(-1)
        kg = k[:, :, idx_c, :].reshape(B, H, S, W, Dh)
        vg = v[:, :, idx_c, :].reshape(B, H, S, W, Dh)
        logits = torch.einsum("bhsd,bhswd->bhsw", q, kg) * scale
        logits = torch.where(mask[None, None, :, :], logits, -1e9)
        attn = _softmax(logits, axis=-1)
        o = torch.einsum("bhsw,bhswd->bhsd", attn, vg)
        return (lambda x, *d: x.permute(*d))(o, 0, 2, 1, 3).reshape(B, S, D) @ wo

    def _B_ref(x, w1, b1, w2, b2):
        h = torch.einsum("bsd,df->bsf", x, w1) + b1
        h = _gelu(h)
        return torch.einsum("bsf,fd->bsd", h, w2) + b2

    x = A_x
    xn = _layernorm(x, axis=-1)
    s = _A_ref(xn, A_wqkv, A_wo)
    g = _silu(_B_ref(xn, B_w1, B_b1, B_w2, B_b2))
    return x + s * g
```

```cpp
#include <hip/hip_runtime.h>
#include <stdint.h>

using bf16x8 = __attribute__((ext_vector_type(8))) short;
using s16x4 = __attribute__((ext_vector_type(4))) short;
using f32x4 = __attribute__((ext_vector_type(4))) float;
using u32x4 = __attribute__((ext_vector_type(4))) unsigned;
using u32x2 = __attribute__((ext_vector_type(2))) unsigned;
typedef __attribute__((address_space(3))) s16x4* lds_s16x4_ptr;

#define DEV __device__ __forceinline__
DEV float bf2f(unsigned u) { return __uint_as_float(u << 16); }
DEV unsigned f2bf(float f) {
  unsigned u = __float_as_uint(f);
  u += 0x7FFFu + ((u >> 16) & 1u);
  return u >> 16;
}
DEV float rbf(float f) { return bf2f(f2bf(f)); }

constexpr int DM = 576, FF = 4608, NQ = 1728;
constexpr int FC = 288;
constexpr int XS = 144, XBUF = 144 * XS;
constexpr int W1BUF = 64 * 576;
constexpr int WQBUF = 64 * 256;
constexpr int SBUF = XBUF + W1BUF + WQBUF;
constexpr int HS = 592;
constexpr int OFF_H = 0;
constexpr int W2BUF = 32 * 1152;
constexpr int OFF_V = 75776;
constexpr int QS = 272;
constexpr int OFF_Q = OFF_V + W2BUF;
constexpr int OFF_ST = 2 * SBUF;
constexpr int LDS1 = 152064;

DEV bf16x8 tr_frag(const char* p0, const char* p1) {
  s16x4 a = __builtin_amdgcn_ds_read_tr16_b64_v4i16((lds_s16x4_ptr)p0);
  s16x4 b = __builtin_amdgcn_ds_read_tr16_b64_v4i16((lds_s16x4_ptr)p1);
  bf16x8 r;
  r[0] = a[0]; r[1] = a[1]; r[2] = a[2]; r[3] = a[3];
  r[4] = b[0]; r[5] = b[1]; r[6] = b[2]; r[7] = b[3];
  return r;
}

DEV float gelu_tanh(float v) {
  float u = 0.7978845608028654f * (v + 0.044715f * v * v * v);
  float t = 1.f - 2.f / (__expf(2.f * u) + 1.f);
  return 0.5f * v * (1.f + t);
}

__global__ __launch_bounds__(512, 2) void k1(const unsigned short* __restrict__ x,
                                             const unsigned short* __restrict__ wqkv,
                                             const unsigned short* __restrict__ w1,
                                             const unsigned short* __restrict__ b1,
                                             const unsigned short* __restrict__ w2,
                                             unsigned short* __restrict__ o_ws,
                                             float* __restrict__ part) {
  extern __shared__ __attribute__((aligned(16))) char lds[];
  const int tid = threadIdx.x, lane = tid & 63, wave = tid >> 6;
  const int bid = blockIdx.x;
  const int mt = (bid >> 3) & 15;
  const int fs = (bid & 7) * 2 + (bid >> 7);
  const int r0 = mt * 128;
  const int wm = wave >> 1, wn = wave & 1;
  const int g = lane >> 4, l15 = lane & 15, q = l15 >> 2, p = lane & 3;
  float* st = (float*)(lds + OFF_ST);

  for (int rg = wave; rg < 18; rg += 8) {
    int row = rg * 8 + (lane >> 3), l8 = lane & 7;
    int gr = r0 - 16 + row; gr = gr < 0 ? 0 : gr;
    const unsigned short* xr = x + (size_t)gr * DM;
    u32x4 v[9];
#pragma unroll
    for (int i = 0; i < 9; ++i) v[i] = *(const u32x4*)(xr + (l8 + 8 * i) * 8);
    float s = 0.f;
#pragma unroll
    for (int i = 0; i < 9; ++i)
#pragma unroll
      for (int j = 0; j < 4; ++j) s += bf2f(v[i][j] & 0xffffu) + bf2f(v[i][j] >> 16);
    s += __shfl_xor(s, 1); s += __shfl_xor(s, 2); s += __shfl_xor(s, 4);
    float mean = s * (1.f / 576.f);
    float ss = 0.f;
#pragma unroll
    for (int i = 0; i < 9; ++i)
#pragma unroll
      for (int j = 0; j < 4; ++j) {
        float a = bf2f(v[i][j] & 0xffffu) - mean, b = bf2f(v[i][j] >> 16) - mean;
        ss += a * a + b * b;
      }
    ss += __shfl_xor(ss, 1); ss += __shfl_xor(ss, 2); ss += __shfl_xor(ss, 4);
    float var = rbf(ss * (1.f / 576.f));
    float den = rbf(sqrtf(rbf(var + 1e-5f)));
    if (l8 == 0) { st[row * 2] = rbf(mean); st[row * 2 + 1] = 1.f / den; }
  }
  __syncthreads();

  const int srow = tid >> 3, sc = tid & 7;
  float xmean[3], xrinv[3];
  int xo[3];
#pragma unroll
  for (int i = 0; i < 3; ++i) {
    int row = srow + 64 * i; if (row >= 144) row = 0;
    int gr = r0 - 16 + row; gr = gr < 0 ? 0 : gr;
    xo[i] = (gr * DM + sc * 8) * 2;
    xmean[i] = st[row * 2]; xrinv[i] = st[row * 2 + 1];
  }
  const int xl0 = srow * XS + sc * 16;
  u32x4 xr_[3];
  int w1o[5], wqo[2];
#pragma unroll
  for (int i = 0; i < 5; ++i) {
    int L = 64 * (wave + 8 * i) + lane, row = L / 36, c = L % 36;
    c ^= 2 * ((row >> 3) & 1);
    w1o[i] = (row * FF + fs * FC + c * 8) * 2;
  }
#pragma unroll
  for (int i = 0; i < 2; ++i) {
    int L = 64 * (wave + 8 * i) + lane, row = L >> 4, c = L & 15;
    c ^= 2 * ((row & 3) | ((row >> 3) & 1) << 2);
    int sg = c / 5, c5 = c % 5; if (sg > 2) { sg = 2; c5 = 0; }
    wqo[i] = (row * NQ + sg * 576 + fs * 36 + c5 * 8) * 2;
  }
  auto stage_w = [&](int s, int b) {
    char* wb = lds + b * SBUF + XBUF;
    const char* w1s = (const char*)w1 + (size_t)s * 64 * FF * 2;
    const char* wqs = (const char*)wqkv + (size_t)s * 64 * NQ * 2;
#pragma unroll
    for (int i = 0; i < 5; ++i)
      if (i < 4 || wave < 4) {
        unsigned off = w1o[i]; asm volatile("" : "+v"(off));
        __builtin_amdgcn_global_load_lds((const unsigned*)(w1s + off), (unsigned*)(wb + (wave + 8 * i) * 1024), 16, 0, 0);
      }
#pragma unroll
    for (int i = 0; i < 2; ++i) {
      unsigned off = wqo[i]; asm volatile("" : "+v"(off));
      if (s == 8 && off > (64 * NQ - 8) * 2) off = (64 * NQ - 8) * 2;
      __builtin_amdgcn_global_load_lds((const unsigned*)(wqs + off), (unsigned*)(wb + W1BUF + (wave + 8 * i) * 1024), 16, 0, 0);
    }
  };
  auto load_x = [&](int s) {
#pragma unroll
    for (int i = 0; i < 3; ++i) if (i < 2 || tid < 128) {
      unsigned off = xo[i]; asm volatile("" : "+v"(off));
      xr_[i] = *(const u32x4*)((const char*)x + s * 128 + off);
    }
  };
  auto write_x = [&](int b) {
    char* xb = lds + b * SBUF + xl0;
#pragma unroll
    for (int i = 0; i < 3; ++i) {
      if (i < 2 || tid < 128) {
        u32x4 o;
#pragma unroll
        for (int j = 0; j < 4; ++j) {
          float a = rbf(bf2f(xr_[i][j] & 0xffffu) - xmean[i]) * xrinv[i];
          float c = rbf(bf2f(xr_[i][j] >> 16) - xmean[i]) * xrinv[i];
          o[j] = f2bf(a) | (f2bf(c) << 16);
        }
        *(u32x4*)(xb + i * 64 * XS) = o;
      }
    }
  };

  f32x4 acc[2][13], acch = f32x4{0.f, 0.f, 0.f, 0.f};
#pragma unroll
  for (int r = 0; r < 2; ++r)
#pragma unroll
    for (int i = 0; i < 13; ++i) acc[r][i] = f32x4{0.f, 0.f, 0.f, 0.f};

  stage_w(0, 0);
  load_x(0);
  write_x(0);
  __syncthreads();
  const int htile = 2 + (wave < 5 ? wave : 5);
#pragma unroll 1
  for (int s = 0; s < 9; ++s) {
    { int sn = s < 8 ? s + 1 : 8; stage_w(sn, (s + 1) & 1); load_x(sn); }
    const char* xb = lds + (s & 1) * SBUF + (16 + 32 * wm + l15) * XS + g * 16;
    const char* wbase = lds + (s & 1) * SBUF + XBUF + (8 * g + q) * 576 + 8 * p;
    const char* wE = wbase + 32 * (g & 1);
    const char* wO = wbase - 32 * (g & 1);
    const char* qbase = lds + (s & 1) * SBUF + XBUF + W1BUF + (8 * g + q) * 256 + 8 * p;
    const int qsw = (q | ((g & 1) << 2));
#define WFRAG(nt, kk) tr_frag(((nt) & 1 ? wO : wE) + 32 * (nt) + (kk) * 32 * 576, ((nt) & 1 ? wO : wE) + 32 * (nt) + (kk) * 32 * 576 + 4 * 576)
#define QFRAG(nt, kk) tr_frag(qbase + 32 * ((nt) ^ qsw) + (kk) * 32 * 256, qbase + 32 * ((nt) ^ qsw) + (kk) * 32 * 256 + 4 * 256)
#pragma unroll
    for (int kk = 0; kk < 2; ++kk) {
      bf16x8 xf0 = *(const bf16x8*)(xb + kk * 64);
      bf16x8 xf1 = *(const bf16x8*)(xb + 16 * XS + kk * 64);
      bf16x8 xh = *(const bf16x8*)(xb - (16 + 32 * wm) * XS + kk * 64);
      if (wn == 0) {
#pragma unroll
        for (int i = 0; i < 9; ++i) {
          bf16x8 wf = WFRAG(i, kk);
          acc[0][i] = __builtin_amdgcn_mfma_f32_16x16x32_bf16(wf, xf0, acc[0][i], 0, 0, 0);
          acc[1][i] = __builtin_amdgcn_mfma_f32_16x16x32_bf16(wf, xf1, acc[1][i], 0, 0, 0);
        }
      } else {
#pragma unroll
        for (int i = 0; i < 9; ++i) {
          bf16x8 wf = WFRAG(9 + i, kk);
          acc[0][i] = __builtin_amdgcn_mfma_f32_16x16x32_bf16(wf, xf0, acc[0][i], 0, 0, 0);
          acc[1][i] = __builtin_amdgcn_mfma_f32_16x16x32_bf16(wf, xf1, acc[1][i], 0, 0, 0);
        }
      }
#pragma unroll
      for (int i = 0; i < 4; ++i) {
        bf16x8 wf = QFRAG(wn * 4 + i, kk);
        acc[0][9 + i] = __builtin_amdgcn_mfma_f32_16x16x32_bf16(wf, xf0, acc[0][9 + i], 0, 0, 0);
        acc[1][9 + i] = __builtin_amdgcn_mfma_f32_16x16x32_bf16(wf, xf1, acc[1][9 + i], 0, 0, 0);
      }
      {
        bf16x8 wf = QFRAG(htile, kk);
        acch = __builtin_amdgcn_mfma_f32_16x16x32_bf16(wf, xh, acch, 0, 0, 0);
      }
    }
    write_x((s + 1) & 1);
    __syncthreads();
  }

  {
    char* hb = lds + OFF_H; char* qb = lds + OFF_Q;
#pragma unroll
    for (int i = 0; i < 9; ++i) {
      int f = (wn * 9 + i) * 16 + 4 * g;
      u32x2 bb = *(const u32x2*)(b1 + fs * FC + f);
      float bv[4] = {bf2f(bb[0] & 0xffffu), bf2f(bb[0] >> 16), bf2f(bb[1] & 0xffffu), bf2f(bb[1] >> 16)};
#pragma unroll
      for (int r = 0; r < 2; ++r) {
        int m = 32 * wm + 16 * r + l15;
        unsigned hv[4];
#pragma unroll
        for (int j = 0; j < 4; ++j) hv[j] = f2bf(gelu_tanh(rbf(rbf(acc[r][i][j]) + bv[j])));
        u32x2 o; o[0] = hv[0] | (hv[1] << 16); o[1] = hv[2] | (hv[3] << 16);
        *(u32x2*)(hb + m * HS + f * 2) = o;
      }
      __builtin_amdgcn_sched_barrier(0);
    }
#pragma unroll
    for (int i = 9; i < 13; ++i)
#pragma unroll
      for (int r = 0; r < 2; ++r) {
        int col = wn * 64 + (i - 9) * 16 + 4 * g, row = 16 + 32 * wm + 16 * r + l15;
        u32x2 o; o[0] = f2bf(acc[r][i][0]) | (f2bf(acc[r][i][1]) << 16); o[1] = f2bf(acc[r][i][2]) | (f2bf(acc[r][i][3]) << 16);
        *(u32x2*)(qb + row * QS + col * 2) = o;
      }
    if (wave < 6) {
      int col = 32 + 16 * wave + 4 * g, row = l15;
      u32x2 o; o[0] = f2bf(acch[0]) | (f2bf(acch[1]) << 16); o[1] = f2bf(acch[2]) | (f2bf(acch[3]) << 16);
      *(u32x2*)(qb + row * QS + col * 2) = o;
    }
  }
  int w2o[5];
#pragma unroll
  for (int i = 0; i < 5; ++i) {
    int L = 64 * (wave + 8 * i) + lane, row = L / 72, c = L % 72;
    c ^= 2 * (((row >> 1) & 1) | (((row >> 3) & 1) << 1));
    w2o[i] = ((fs * FC + row) * DM + c * 8) * 2;
  }
  auto stage2 = [&](int s, int b) {
    char* vb = lds + OFF_V + b * W2BUF;
    const char* w2s = (const char*)w2 + (size_t)s * 32 * DM * 2;
#pragma unroll
    for (int i = 0; i < 5; ++i)
      if (i < 4 || wave < 4) {
        unsigned off = w2o[i]; asm volatile("" : "+v"(off));
        __builtin_amdgcn_global_load_lds((const unsigned*)(w2s + off), (unsigned*)(vb + (wave + 8 * i) * 1024), 16, 0, 0);
      }
  };
  stage2(0, 0);
  __syncthreads();

  if (tid < 256) {
    const char* qb = lds + OFF_Q;
    int row = tid >> 1, hh = tid & 1;
    const unsigned* qp = (const unsigned*)(qb + (16 + row) * QS + 36 * hh);
    float qv[18];
#pragma unroll
    for (int d = 0; d < 9; ++d) { unsigned u = qp[d]; qv[2 * d] = bf2f(u & 0xffffu); qv[2 * d + 1] = bf2f(u >> 16); }
    float lg[8];
#pragma unroll
    for (int w = 0; w < 8; ++w) {
      const unsigned* kp = (const unsigned*)(qb + (16 + row - 2 * w) * QS + 80 + 36 * hh);
      float a = 0.f;
#pragma unroll
      for (int d = 0; d < 9; ++d) { unsigned u = kp[d]; a += qv[2 * d] * bf2f(u & 0xffffu); a += qv[2 * d + 1] * bf2f(u >> 16); }
      a = rbf(rbf(a) * 0.23570226039551584f);
      lg[w] = (r0 + row - 2 * w >= 0) ? a : -1e9f;
    }
    float mx = lg[0];
#pragma unroll
    for (int w = 1; w < 8; ++w) mx = fmaxf(mx, lg[w]);
    float sum = 0.f;
#pragma unroll
    for (int w = 0; w < 8; ++w) { lg[w] = __expf(lg[w] - mx); sum += lg[w]; }
    float rs = 1.f / sum;
    float ov[18];
#pragma unroll
    for (int d = 0; d < 18; ++d) ov[d] = 0.f;
#pragma unroll
    for (int w = 0; w < 8; ++w) {
      float pw = rbf(lg[w] * rs);
      const unsigned* vp = (const unsigned*)(qb + (16 + row - 2 * w) * QS + 160 + 36 * hh);
#pragma unroll
      for (int d = 0; d < 9; ++d) { unsigned u = vp[d]; ov[2 * d] += pw * bf2f(u & 0xffffu); ov[2 * d + 1] += pw * bf2f(u >> 16); }
    }
    unsigned* op = (unsigned*)(o_ws + (size_t)(r0 + row) * DM + fs * 36 + 18 * hh);
#pragma unroll
    for (int d = 0; d < 9; ++d) op[d] = f2bf(ov[2 * d]) | (f2bf(ov[2 * d + 1]) << 16);
  }
  __syncthreads();

  f32x4 acc2[2][18];
#pragma unroll
  for (int r = 0; r < 2; ++r)
#pragma unroll
    for (int i = 0; i < 18; ++i) acc2[r][i] = f32x4{0.f, 0.f, 0.f, 0.f};
#pragma unroll 1
  for (int s = 0; s < 9; ++s) {
    stage2(s < 8 ? s + 1 : 8, (s + 1) & 1);
    const char* vb = lds + OFF_V + (s & 1) * W2BUF;
    const char* hb = lds + OFF_H;
    bf16x8 hf0 = *(const bf16x8*)(hb + (32 * wm + l15) * HS + s * 64 + g * 16);
    bf16x8 hf1 = *(const bf16x8*)(hb + (32 * wm + 16 + l15) * HS + s * 64 + g * 16);
    const int sq = ((q >> 1) & 1) | ((g & 1) << 1);
    const char* vbase = vb + (8 * g + q) * 1152 + 8 * p;
    const char* vB[4] = {vbase + 32 * (0 ^ sq), vbase + 32 * (1 ^ sq), vbase + 32 * (2 ^ sq), vbase + 32 * (3 ^ sq)};
    if (wn == 0) {
#pragma unroll
      for (int i = 0; i < 18; ++i) {
        const char* a0 = vB[i & 3] + 128 * (i >> 2);
        bf16x8 wf = tr_frag(a0, a0 + 4 * 1152);
        acc2[0][i] = __builtin_amdgcn_mfma_f32_16x16x32_bf16(wf, hf0, acc2[0][i], 0, 0, 0);
        acc2[1][i] = __builtin_amdgcn_mfma_f32_16x16x32_bf16(wf, hf1, acc2[1][i], 0, 0, 0);
      }
    } else {
#pragma unroll
      for (int i = 0; i < 18; ++i) {
        const int nt = 18 + i;
        const char* a0 = vB[nt & 3] + 128 * (nt >> 2);
        bf16x8 wf = tr_frag(a0, a0 + 4 * 1152);
        acc2[0][i] = __builtin_amdgcn_mfma_f32_16x16x32_bf16(wf, hf0, acc2[0][i], 0, 0, 0);
        acc2[1][i] = __builtin_amdgcn_mfma_f32_16x16x32_bf16(wf, hf1, acc2[1][i], 0, 0, 0);
      }
    }
    __syncthreads();
  }
#pragma unroll
  for (int r = 0; r < 2; ++r)
#pragma unroll
    for (int i = 0; i < 18; ++i) {
      int n = (18 * wn + i) * 16 + 4 * g, m = 32 * wm + 16 * r + l15;
      *(f32x4*)(part + ((size_t)fs * 2048 + r0 + m) * DM + n) = acc2[r][i];
    }
}

constexpr int W3S = 320, W3BUF = 64 * W3S;
__global__ __launch_bounds__(256) void k2(const unsigned short* __restrict__ x,
                                          const unsigned short* __restrict__ wo,
                                          const unsigned short* __restrict__ b2,
                                          const unsigned short* __restrict__ o_ws,
                                          const float* __restrict__ part,
                                          unsigned short* __restrict__ out) {
  __shared__ __attribute__((aligned(16))) char lds[W3BUF];
  const int tid = threadIdx.x, lane = tid & 63, wave = tid >> 6;
  const int row0 = (blockIdx.x >> 2) * 32, col0 = (blockIdx.x & 3) * 144;
  const int g = lane >> 4, l15 = lane & 15, q = l15 >> 2, p = lane & 3;
  const int r = wave & 1, nh = wave >> 1;
  f32x4 acc[5];
#pragma unroll
  for (int i = 0; i < 5; ++i) acc[i] = f32x4{0.f, 0.f, 0.f, 0.f};
  const unsigned short* orow = o_ws + (size_t)(row0 + 16 * r + l15) * DM + 8 * g;
  for (int s = 0; s < 9; ++s) {
    __syncthreads();
#pragma unroll
    for (int i = 0; i < 5; ++i) {
      int ch = tid + 256 * i;
      if (ch < 1152) {
        int row = ch / 18, c = ch % 18;
        u32x4 v = *(const u32x4*)(wo + (size_t)(s * 64 + row) * DM + col0 + c * 8);
        *(u32x4*)(lds + row * W3S + ((16 * c) ^ (32 * ((row >> 3) & 1)))) = v;
      }
    }
    __syncthreads();
#pragma unroll
    for (int kk = 0; kk < 2; ++kk) {
      bf16x8 of = *(const bf16x8*)(orow + s * 64 + kk * 32);
      const char* wrow0 = lds + (32 * kk + 8 * g + q) * W3S;
      const int sw = 32 * (g & 1);
#pragma unroll
      for (int i = 0; i < 5; ++i) {
        if (i < 4 || nh == 0) {
          int nt = nh * 5 + i;
          const char* a0 = wrow0 + ((32 * nt + 8 * p) ^ sw);
          bf16x8 wf = tr_frag(a0, a0 + 4 * W3S);
          acc[i] = __builtin_amdgcn_mfma_f32_16x16x32_bf16(wf, of, acc[i], 0, 0, 0);
        }
      }
    }
  }
#pragma unroll
  for (int i = 0; i < 5; ++i) {
    if (i < 4 || nh == 0) {
      int n = col0 + (nh * 5 + i) * 16 + 4 * g, m = row0 + 16 * r + l15;
      f32x4 y = f32x4{0.f, 0.f, 0.f, 0.f};
      for (int f = 0; f < 16; ++f) y += *(const f32x4*)(part + ((size_t)f * 2048 + m) * DM + n);
      u32x2 bb = *(const u32x2*)(b2 + n);
      u32x2 xx = *(const u32x2*)(x + (size_t)m * DM + n);
      float bv[4] = {bf2f(bb[0] & 0xffffu), bf2f(bb[0] >> 16), bf2f(bb[1] & 0xffffu), bf2f(bb[1] >> 16)};
      float xv[4] = {bf2f(xx[0] & 0xffffu), bf2f(xx[0] >> 16), bf2f(xx[1] & 0xffffu), bf2f(xx[1] >> 16)};
      unsigned ov[4];
#pragma unroll
      for (int j = 0; j < 4; ++j) {
        float yy = rbf(rbf(y[j]) + bv[j]);
        float gg = rbf(yy / (1.f + __expf(-yy)));
        float sv = rbf(acc[i][j]);
        ov[j] = f2bf(xv[j] + rbf(sv * gg));
      }
      u32x2 o; o[0] = ov[0] | (ov[1] << 16); o[1] = ov[2] | (ov[3] << 16);
      *(u32x2*)(out + (size_t)m * DM + n) = o;
    }
  }
}

extern "C" void kernel_launch(void* const* d_in, const int* in_sizes, int n_in,
                              void* d_out, int out_size, void* d_ws, size_t ws_size,
                              hipStream_t stream) {
  const unsigned short* x = (const unsigned short*)d_in[0];
  const unsigned short* wqkv = (const unsigned short*)d_in[1];
  const unsigned short* wo = (const unsigned short*)d_in[2];
  const unsigned short* w1 = (const unsigned short*)d_in[4];
  const unsigned short* b1 = (const unsigned short*)d_in[5];
  const unsigned short* w2 = (const unsigned short*)d_in[6];
  const unsigned short* b2 = (const unsigned short*)d_in[7];
  unsigned short* out = (unsigned short*)d_out;
  unsigned short* o_ws = (unsigned short*)d_ws;
  float* part = (float*)((char*)d_ws + (4u << 20));
  static bool attr = false;
  if (!attr) { hipFuncSetAttribute((const void*)k1, hipFuncAttributeMaxDynamicSharedMemorySize, LDS1); attr = true; }
  k1<<<256, 512, LDS1, stream>>>(x, wqkv, w1, b1, w2, o_ws, part);
  k2<<<256, 256, 0, stream>>>(x, wo, b2, o_ws, part, out);
}
```

```cpp
#include <hip/hip_runtime.h>
#include <stdint.h>

using bf16x8 = __attribute__((ext_vector_type(8))) short;
using s16x4 = __attribute__((ext_vector_type(4))) short;
using f32x4 = __attribute__((ext_vector_type(4))) float;
using u32x4 = __attribute__((ext_vector_type(4))) unsigned;
using u32x2 = __attribute__((ext_vector_type(2))) unsigned;
typedef __attribute__((address_space(3))) s16x4* lds_s16x4_ptr;

#define DEV __device__ __forceinline__
DEV float bf2f(unsigned u) { return __uint_as_float(u << 16); }
DEV unsigned f2bf(float f) {
  unsigned u = __float_as_uint(f);
  u += 0x7FFFu + ((u >> 16) & 1u);
  return u >> 16;
}
DEV float rbf(float f) { return bf2f(f2bf(f)); }

constexpr int DM = 576, FF = 4608, NQ = 1728;
constexpr int FC = 288;
constexpr int SW1 = 18432, SWQ = 8192, SX = 9216, SLOT1 = SW1 + SWQ + SX;
constexpr int HS = 592;
constexpr int OFF_H = 0;
constexpr int QS = 272;
constexpr int OFF_Q = 75776;
constexpr int SLOT3 = 18432;
constexpr int OFF_B1 = 147456;
constexpr int LDS1 = 148608;

DEV bf16x8 tr_frag(const char* p0, const char* p1) {
  s16x4 a = __builtin_amdgcn_ds_read_tr16_b64_v4i16((lds_s16x4_ptr)p0);
  s16x4 b = __builtin_amdgcn_ds_read_tr16_b64_v4i16((lds_s16x4_ptr)p1);
  bf16x8 r;
  r[0] = a[0]; r[1] = a[1]; r[2] = a[2]; r[3] = a[3];
  r[4] = b[0]; r[5] = b[1]; r[6] = b[2]; r[7] = b[3];
  return r;
}
DEV void glds16(const void* sbase, unsigned voff, unsigned lds_dst) {
  unsigned keep;
  asm volatile("s_mov_b32 %0, m0\n\ts_mov_b32 m0, %3\n\ts_nop 0\n\tglobal_load_lds_dwordx4 %1, %2\n\ts_mov_b32 m0, %0"
               : "=&s"(keep) : "v"(voff), "s"(sbase), "s"(lds_dst) : "memory");
}
#define VMWAIT(n) asm volatile("s_waitcnt vmcnt(" #n ")" ::: "memory")
#define BARRIER() do { asm volatile("s_waitcnt lgkmcnt(0)" ::: "memory"); __builtin_amdgcn_s_barrier(); asm volatile("" ::: "memory"); } while (0)

DEV float gelu_tanh(float v) {
  float a = v * v * (0.044715f * 2.f * 0.7978845608028654f) + 2.f * 0.7978845608028654f;
  return v * __builtin_amdgcn_rcpf(1.f + __expf(-a * v));
}

__global__ __launch_bounds__(256) void k0(const unsigned short* __restrict__ x, unsigned short* __restrict__ xn) {
  const int lane = threadIdx.x & 63, l8 = lane & 7;
  const int row = blockIdx.x * 32 + (threadIdx.x >> 3);
  const unsigned short* xr = x + (size_t)row * DM;
  u32x4 v[9];
#pragma unroll
  for (int i = 0; i < 9; ++i) v[i] = *(const u32x4*)(xr + (l8 + 8 * i) * 8);
  float s = 0.f;
#pragma unroll
  for (int i = 0; i < 9; ++i)
#pragma unroll
    for (int j = 0; j < 4; ++j) s += bf2f(v[i][j] & 0xffffu) + bf2f(v[i][j] >> 16);
  s += __shfl_xor(s, 1); s += __shfl_xor(s, 2); s += __shfl_xor(s, 4);
  float mean = s * (1.f / 576.f);
  float ss = 0.f;
#pragma unroll
  for (int i = 0; i < 9; ++i)
#pragma unroll
    for (int j = 0; j < 4; ++j) {
      float a = bf2f(v[i][j] & 0xffffu) - mean, b = bf2f(v[i][j] >> 16) - mean;
      ss += a * a + b * b;
    }
  ss += __shfl_xor(ss, 1); ss += __shfl_xor(ss, 2); ss += __shfl_xor(ss, 4);
  float var = rbf(ss * (1.f / 576.f));
  float den = rbf(sqrtf(rbf(var + 1e-5f)));
  float mb = rbf(mean);
#pragma unroll
  for (int i = 0; i < 9; ++i) {
    u32x4 o;
#pragma unroll
    for (int j = 0; j < 4; ++j) {
      float a = rbf(bf2f(v[i][j] & 0xffffu) - mb) / den;
      float c = rbf(bf2f(v[i][j] >> 16) - mb) / den;
      o[j] = f2bf(a) | (f2bf(c) << 16);
    }
    *(u32x4*)(xn + (size_t)row * DM + (l8 + 8 * i) * 8) = o;
  }
}

__global__ __launch_bounds__(512, 2) void k1(const unsigned short* __restrict__ xn,
                                             const unsigned short* __restrict__ wqkv,
                                             const unsigned short* __restrict__ w1,
                                             const unsigned short* __restrict__ b1,
                                             const unsigned short* __restrict__ w2,
                                             unsigned short* __restrict__ o_ws,
                                             float* __restrict__ part) {
  extern __shared__ __attribute__((aligned(16))) char lds[];
  const int tid = threadIdx.x, lane = tid & 63, wave = __builtin_amdgcn_readfirstlane(tid >> 6);
  const int bid = blockIdx.x;
  const int mt = (bid >> 3) & 15;
  const int fs = (bid & 7) * 2 + (bid >> 7);
  const int r0 = mt * 128;
  const int wm = wave >> 1, wn = wave & 1;
  const int g = lane >> 4, l15 = lane & 15, q = l15 >> 2, p = lane & 3;
  const unsigned ldsb = (unsigned)(size_t)lds + (unsigned)wave * 1024u;
#define STAMP(i)
  if (tid < 288) ((float*)(lds + OFF_B1))[tid] = bf2f(b1[fs * FC + tid]);

  unsigned so[5];
  {
    auto w1off = [&](int pc) { int L = 64 * pc + lane, row = L / 36, c = L % 36; c ^= 2 * ((row >> 3) & 1); return (unsigned)((row * FF + fs * FC + c * 8) * 2); };
    auto wqoff = [&](int pc) { int L = 64 * pc + lane, row = L >> 4, c = L & 15; c ^= 2 * ((row & 3) | ((row >> 3) & 1) << 2);
                               int sg = c / 5, c5 = c % 5; if (sg > 2) { sg = 2; c5 = 0; } return (unsigned)((row * NQ + sg * 576 + fs * 36 + c5 * 8) * 2); };
    auto xoff = [&](int pc) { int L = 64 * pc + lane, row = L >> 2, c = (L & 3) ^ ((row >> 2) & 3);
                              int gr = r0 - 16 + row; gr = gr < 0 ? 0 : gr; return (unsigned)((gr * DM + c * 8) * 2); };
    so[0] = w1off(wave); so[1] = w1off(wave + 8);
    so[2] = wave < 2 ? w1off(16 + wave) : wqoff(wave - 2);
    so[3] = wave < 2 ? wqoff(6 + wave) : xoff(wave - 2);
    so[4] = wave < 3 ? xoff(6 + wave) : xoff(wave - 3);
  }
  auto stage1 = [&](int s) {
    const unsigned sb = (unsigned)(size_t)lds + (s & 3) * SLOT1;
    const char* w1s = (const char*)w1 + (size_t)s * 32 * FF * 2;
    const char* wqs = (const char*)wqkv + (size_t)s * 32 * NQ * 2;
    const char* xs = (const char*)xn + s * 64;
    glds16(w1s, so[0], sb + wave * 1024);
    glds16(w1s, so[1], sb + (wave + 8) * 1024);
    if (wave < 2) glds16(w1s, so[2], sb + (16 + wave) * 1024);
    else glds16(wqs, so[2], sb + SW1 + (wave - 2) * 1024);
    if (wave < 2) { unsigned o3 = so[3]; if (s == 17 && o3 > (32 * NQ - 8) * 2) o3 = (32 * NQ - 8) * 2; glds16(wqs, o3, sb + SW1 + (6 + wave) * 1024); }
    else glds16(xs, so[3], sb + SW1 + SWQ + (wave - 2) * 1024);
    glds16(xs, so[4], sb + SW1 + SWQ + (wave < 3 ? 6 + wave : wave - 3) * 1024);
  };

  f32x4 acc[2][13], acch = f32x4{0.f, 0.f, 0.f, 0.f};
#pragma unroll
  for (int r = 0; r < 2; ++r)
#pragma unroll
    for (int i = 0; i < 13; ++i) acc[r][i] = f32x4{0.f, 0.f, 0.f, 0.f};

  stage1(0); stage1(1); stage1(2);
  const int htile = 2 + (wave < 5 ? wave : 5);
#pragma unroll 1
  for (int s = 0; s < 18; ++s) {
    VMWAIT(10);
    BARRIER();
    stage1(s + 3 < 18 ? s + 3 : 17);
    const char* sl = lds + (s & 3) * SLOT1;
    const char* xb = sl + SW1 + SWQ + (16 + 32 * wm + l15) * 64 + ((g ^ (l15 >> 2)) * 16);
    const char* wbase = sl + (8 * g + q) * 576 + 8 * p;
    const char* wE = wbase + 32 * (g & 1);
    const char* wO = wbase - 32 * (g & 1);
    const char* qbase = sl + SW1 + (8 * g + q) * 256 + 8 * p;
    const int qsw = (q | ((g & 1) << 2));
#define WFRAG(nt) tr_frag(((nt) & 1 ? wO : wE) + 32 * (nt), ((nt) & 1 ? wO : wE) + 32 * (nt) + 4 * 576)
#define QFRAG(nt) tr_frag(qbase + 32 * ((nt) ^ qsw), qbase + 32 * ((nt) ^ qsw) + 4 * 256)
    bf16x8 xf0 = *(const bf16x8*)(xb);
    bf16x8 xf1 = *(const bf16x8*)(xb + 16 * 64);
    bf16x8 xh = *(const bf16x8*)(xb - (16 + 32 * wm) * 64);
    if (wn == 0) {
#pragma unroll
      for (int i = 0; i < 9; ++i) {
        bf16x8 wf = WFRAG(i);
        acc[0][i] = __builtin_amdgcn_mfma_f32_16x16x32_bf16(wf, xf0, acc[0][i], 0, 0, 0);
        acc[1][i] = __builtin_amdgcn_mfma_f32_16x16x32_bf16(wf, xf1, acc[1][i], 0, 0, 0);
      }
    } else {
#pragma unroll
      for (int i = 0; i < 9; ++i) {
        bf16x8 wf = WFRAG(9 + i);
        acc[0][i] = __builtin_amdgcn_mfma_f32_16x16x32_bf16(wf, xf0, acc[0][i], 0, 0, 0);
        acc[1][i] = __builtin_amdgcn_mfma_f32_16x16x32_bf16(wf, xf1, acc[1][i], 0, 0, 0);
      }
    }
#pragma unroll
    for (int i = 0; i < 4; ++i) {
      bf16x8 wf = QFRAG(wn * 4 + i);
      acc[0][9 + i] = __builtin_amdgcn_mfma_f32_16x16x32_bf16(wf, xf0, acc[0][9 + i], 0, 0, 0);
      acc[1][9 + i] = __builtin_amdgcn_mfma_f32_16x16x32_bf16(wf, xf1, acc[1][9 + i], 0, 0, 0);
    }
    {
      bf16x8 wf = QFRAG(htile);
      acch = __builtin_amdgcn_mfma_f32_16x16x32_bf16(wf, xh, acch, 0, 0, 0);
    }
  }
  VMWAIT(0);
  BARRIER();

  STAMP(1)
  {
    char* hb = lds + OFF_H; char* qb = lds + OFF_Q;
#pragma unroll
    for (int i = 0; i < 9; ++i) {
      int f = (wn * 9 + i) * 16 + 4 * g;
      f32x4 bv = *(const f32x4*)(lds + OFF_B1 + f * 4);
#pragma unroll
      for (int r = 0; r < 2; ++r) {
        int m = 32 * wm + 16 * r + l15;
        unsigned hv[4];
#pragma unroll
        for (int j = 0; j < 4; ++j) hv[j] = f2bf(gelu_tanh(rbf(rbf(acc[r][i][j]) + bv[j])));
        u32x2 o; o[0] = hv[0] | (hv[1] << 16); o[1] = hv[2] | (hv[3] << 16);
        *(u32x2*)(hb + m * HS + f * 2) = o;
      }
    }
#pragma unroll
    for (int i = 9; i < 13; ++i)
#pragma unroll
      for (int r = 0; r < 2; ++r) {
        int col = wn * 64 + (i - 9) * 16 + 4 * g, row = 16 + 32 * wm + 16 * r + l15;
        u32x2 o; o[0] = f2bf(acc[r][i][0]) | (f2bf(acc[r][i][1]) << 16); o[1] = f2bf(acc[r][i][2]) | (f2bf(acc[r][i][3]) << 16);
        *(u32x2*)(qb + row * QS + col * 2) = o;
      }
    if (wave < 6) {
      int col = 32 + 16 * wave + 4 * g, row = l15;
      u32x2 o; o[0] = f2bf(acch[0]) | (f2bf(acch[1]) << 16); o[1] = f2bf(acch[2]) | (f2bf(acch[3]) << 16);
      *(u32x2*)(qb + row * QS + col * 2) = o;
    }
  }
  unsigned vo[3];
#pragma unroll
  for (int i = 0; i < 3; ++i) {
    int pc = wave + 8 * i; if (pc >= 18) pc -= 6;
    int L = 64 * pc + lane, row = L / 36, c = L % 36; c ^= 2 * ((row >> 3) & 1);
    vo[i] = (unsigned)(((fs * FC + row) * DM + c * 8) * 2);
  }
  auto stage3 = [&](int s) {
    const int nh = s >= 9, ks = s - 9 * nh;
    const unsigned sb = (unsigned)(size_t)lds + (s & 7) * SLOT3;
    const char* w2s = (const char*)w2 + (size_t)ks * 32 * DM * 2 + nh * 576;
    glds16(w2s, vo[0], sb + wave * 1024);
    glds16(w2s, vo[1], sb + (wave + 8) * 1024);
    glds16(w2s, vo[2], sb + (wave < 2 ? wave + 16 : wave + 10) * 1024);
  };
  BARRIER();
  STAMP(2)

  bf16x8 hf[2][9];
  {
    const char* hb = lds + OFF_H;
#pragma unroll
    for (int ks = 0; ks < 9; ++ks) {
      hf[0][ks] = *(const bf16x8*)(hb + (32 * wm + l15) * HS + ks * 64 + g * 16);
      hf[1][ks] = *(const bf16x8*)(hb + (32 * wm + 16 + l15) * HS + ks * 64 + g * 16);
    }
  }
  if (tid < 256) {
    const char* qb = lds + OFF_Q;
    int row = tid >> 1, hh = tid & 1;
    const unsigned* qp = (const unsigned*)(qb + (16 + row) * QS + 36 * hh);
    float qv[18];
#pragma unroll
    for (int d = 0; d < 9; ++d) { unsigned u = qp[d]; qv[2 * d] = bf2f(u & 0xffffu); qv[2 * d + 1] = bf2f(u >> 16); }
    float lg[8];
#pragma unroll
    for (int w = 0; w < 8; ++w) {
      const unsigned* kp = (const unsigned*)(qb + (16 + row - 2 * w) * QS + 80 + 36 * hh);
      float a = 0.f;
#pragma unroll
      for (int d = 0; d < 9; ++d) { unsigned u = kp[d]; a += qv[2 * d] * bf2f(u & 0xffffu); a += qv[2 * d + 1] * bf2f(u >> 16); }
      a = rbf(rbf(a) * 0.23570226039551584f);
      lg[w] = (r0 + row - 2 * w >= 0) ? a : -1e9f;
    }
    float mx = lg[0];
#pragma unroll
    for (int w = 1; w < 8; ++w) mx = fmaxf(mx, lg[w]);
    float sum = 0.f;
#pragma unroll
    for (int w = 0; w < 8; ++w) { lg[w] = __expf(lg[w] - mx); sum += lg[w]; }
    float rs = 1.f / sum;
    float ov[18];
#pragma unroll
    for (int d = 0; d < 18; ++d) ov[d] = 0.f;
#pragma unroll
    for (int w = 0; w < 8; ++w) {
      float pw = rbf(lg[w] * rs);
      const unsigned* vp = (const unsigned*)(qb + (16 + row - 2 * w) * QS + 160 + 36 * hh);
#pragma unroll
      for (int d = 0; d < 9; ++d) { unsigned u = vp[d]; ov[2 * d] += pw * bf2f(u & 0xffffu); ov[2 * d + 1] += pw * bf2f(u >> 16); }
    }
    unsigned* op = (unsigned*)(o_ws + (size_t)(r0 + row) * DM + fs * 36 + 18 * hh);
#pragma unroll
    for (int d = 0; d < 9; ++d) op[d] = f2bf(ov[2 * d]) | (f2bf(ov[2 * d + 1]) << 16);
  }
  VMWAIT(0);
  BARRIER();
  STAMP(3)

#pragma unroll
  for (int i = 0; i < 7; ++i) stage3(i);
  f32x4 acc2[2][9];
#pragma unroll 1
  for (int nh = 0; nh < 2; ++nh) {
#pragma unroll
    for (int r = 0; r < 2; ++r)
#pragma unroll
      for (int i = 0; i < 9; ++i) acc2[r][i] = f32x4{0.f, 0.f, 0.f, 0.f};
#pragma unroll
    for (int ks = 0; ks < 9; ++ks) {
      const int s = nh * 9 + ks;
      VMWAIT(18);
      BARRIER();
      stage3(s + 7 < 18 ? s + 7 : 17);
      const char* sl = lds + (s & 7) * SLOT3;
      const char* wbase = sl + (8 * g + q) * 576 + 8 * p + wn * 288;
      const char* wE = wbase + 32 * (g & 1);
      const char* wO = wbase - 32 * (g & 1);
      if (wn == 0) {
#pragma unroll
        for (int i = 0; i < 9; ++i) {
          const char* a0 = ((i & 1) ? wO : wE) + 32 * i;
          bf16x8 wf = tr_frag(a0, a0 + 4 * 576);
          acc2[0][i] = __builtin_amdgcn_mfma_f32_16x16x32_bf16(wf, hf[0][ks], acc2[0][i], 0, 0, 0);
          acc2[1][i] = __builtin_amdgcn_mfma_f32_16x16x32_bf16(wf, hf[1][ks], acc2[1][i], 0, 0, 0);
        }
      } else {
#pragma unroll
        for (int i = 0; i < 9; ++i) {
          const char* a0 = ((i & 1) ? wE : wO) + 32 * i;
          bf16x8 wf = tr_frag(a0, a0 + 4 * 576);
          acc2[0][i] = __builtin_amdgcn_mfma_f32_16x16x32_bf16(wf, hf[0][ks], acc2[0][i], 0, 0, 0);
          acc2[1][i] = __builtin_amdgcn_mfma_f32_16x16x32_bf16(wf, hf[1][ks], acc2[1][i], 0, 0, 0);
        }
      }
    }
#pragma unroll
    for (int r = 0; r < 2; ++r)
#pragma unroll
      for (int i = 0; i < 9; ++i) {
        int n = nh * 288 + (9 * wn + i) * 16 + 4 * g, m = 32 * wm + 16 * r + l15;
        *(f32x4*)(part + ((size_t)fs * 2048 + r0 + m) * DM + n) = acc2[r][i];
      }
  }
}

constexpr int W3S = 320, W3BUF = 64 * W3S;
__global__ __launch_bounds__(256) void k2(const unsigned short* __restrict__ x,
                                          const unsigned short* __restrict__ wo,
                                          const unsigned short* __restrict__ b2,
                                          const unsigned short* __restrict__ o_ws,
                                          const float* __restrict__ part,
                                          unsigned short* __restrict__ out) {
  __shared__ __attribute__((aligned(16))) char lds[W3BUF];
  const int tid = threadIdx.x, lane = tid & 63, wave = tid >> 6;
  const int row0 = (blockIdx.x >> 2) * 32, col0 = (blockIdx.x & 3) * 144;
  const int g = lane >> 4, l15 = lane & 15, q = l15 >> 2, p = lane & 3;
  const int r = wave & 1, nh = wave >> 1;
  f32x4 acc[5];
#pragma unroll
  for (int i = 0; i < 5; ++i) acc[i] = f32x4{0.f, 0.f, 0.f, 0.f};
  const unsigned short* orow = o_ws + (size_t)(row0 + 16 * r + l15) * DM + 8 * g;
  for (int s = 0; s < 9; ++s) {
    __syncthreads();
#pragma unroll
    for (int i = 0; i < 5; ++i) {
      int ch = tid + 256 * i;
      if (ch < 1152) {
        int row = ch / 18, c = ch % 18;
        u32x4 v = *(const u32x4*)(wo + (size_t)(s * 64 + row) * DM + col0 + c * 8);
        *(u32x4*)(lds + row * W3S + ((16 * c) ^ (32 * ((row >> 3) & 1)))) = v;
      }
    }
    __syncthreads();
#pragma unroll
    for (int kk = 0; kk < 2; ++kk) {
      bf16x8 of = *(const bf16x8*)(orow + s * 64 + kk * 32);
      const char* wrow0 = lds + (32 * kk + 8 * g + q) * W3S;
      const int sw = 32 * (g & 1);
#pragma unroll
      for (int i = 0; i < 5; ++i) {
        if (i < 4 || nh == 0) {
          int nt = nh * 5 + i;
          const char* a0 = wrow0 + ((32 * nt + 8 * p) ^ sw);
          bf16x8 wf = tr_frag(a0, a0 + 4 * W3S);
          acc[i] = __builtin_amdgcn_mfma_f32_16x16x32_bf16(wf, of, acc[i], 0, 0, 0);
        }
      }
    }
  }
#pragma unroll
  for (int i = 0; i < 5; ++i) {
    if (i < 4 || nh == 0) {
      int n = col0 + (nh * 5 + i) * 16 + 4 * g, m = row0 + 16 * r + l15;
      f32x4 y = f32x4{0.f, 0.f, 0.f, 0.f};
      for (int f = 0; f < 16; ++f) y += *(const f32x4*)(part + ((size_t)f * 2048 + m) * DM + n);
      u32x2 bb = *(const u32x2*)(b2 + n);
      u32x2 xx = *(const u32x2*)(x + (size_t)m * DM + n);
      float bv[4] = {bf2f(bb[0] & 0xffffu), bf2f(bb[0] >> 16), bf2f(bb[1] & 0xffffu), bf2f(bb[1] >> 16)};
      float xv[4] = {bf2f(xx[0] & 0xffffu), bf2f(xx[0] >> 16), bf2f(xx[1] & 0xffffu), bf2f(xx[1] >> 16)};
      unsigned ov[4];
#pragma unroll
      for (int j = 0; j < 4; ++j) {
        float yy = rbf(rbf(y[j]) + bv[j]);
        float gg = rbf(yy / (1.f + __expf(-yy)));
        float sv = rbf(acc[i][j]);
        ov[j] = f2bf(xv[j] + rbf(sv * gg));
      }
      u32x2 o; o[0] = ov[0] | (ov[1] << 16); o[1] = ov[2] | (ov[3] << 16);
      *(u32x2*)(out + (size_t)m * DM + n) = o;
    }
  }
}

extern "C" void kernel_launch(void* const* d_in, const int* in_sizes, int n_in,
                              void* d_out, int out_size, void* d_ws, size_t ws_size,
                              hipStream_t stream) {
  const unsigned short* x = (const unsigned short*)d_in[0];
  const unsigned short* wqkv = (const unsigned short*)d_in[1];
  const unsigned short* wo = (const unsigned short*)d_in[2];
  const unsigned short* w1 = (const unsigned short*)d_in[4];
  const unsigned short* b1 = (const unsigned short*)d_in[5];
  const unsigned short* w2 = (const unsigned short*)d_in[6];
  const unsigned short* b2 = (const unsigned short*)d_in[7];
  unsigned short* out = (unsigned short*)d_out;
  unsigned short* o_ws = (unsigned short*)d_ws;
  unsigned short* xn = (unsigned short*)((char*)d_ws + (200u << 20));
  float* part = (float*)((char*)d_ws + (4u << 20));
  static bool attr = false;
  if (!attr) { hipFuncSetAttribute((const void*)k1, hipFuncAttributeMaxDynamicSharedMemorySize, LDS1); attr = true; }
  k0<<<64, 256, 0, stream>>>(x, xn);
  k1<<<256, 512, LDS1, stream>>>(xn, wqkv, w1, b1, w2, o_ws, part);
  k2<<<256, 256, 0, stream>>>(x, wo, b2, o_ws, part, out);
}
```

```cpp
#include <hip/hip_runtime.h>
#include <stdint.h>

using bf16x8 = __attribute__((ext_vector_type(8))) short;
using s16x4 = __attribute__((ext_vector_type(4))) short;
using f32x4 = __attribute__((ext_vector_type(4))) float;
using u32x4 = __attribute__((ext_vector_type(4))) unsigned;
using u32x2 = __attribute__((ext_vector_type(2))) unsigned;
typedef __attribute__((address_space(3))) s16x4* lds_s16x4_ptr;

#define DEV __device__ __forceinline__
DEV float bf2f(unsigned u) { return __uint_as_float(u << 16); }
DEV unsigned f2bf(float f) {
  unsigned u = __float_as_uint(f);
  u += 0x7FFFu + ((u >> 16) & 1u);
  return u >> 16;
}
DEV float rbf(float f) { return bf2f(f2bf(f)); }
DEV unsigned pk_bf16(float lo, float hi) { unsigned r; asm("v_cvt_pk_bf16_f32 %0, %1, %2" : "=v"(r) : "v"(lo), "v"(hi)); return r; }
using f32x2 = __attribute__((ext_vector_type(2))) float;
DEV f32x2 unpk(unsigned u) { f32x2 r; r[0] = __uint_as_float(u << 16); r[1] = __uint_as_float(u & 0xffff0000u); return r; }

constexpr int DM = 576, FF = 4608, NQ = 1728;
constexpr int FC = 288;
constexpr int SW1 = 18432, SWQ = 8192, SX = 9216, SLOT1 = SW1 + SWQ + SX;
constexpr int HS = 592;
constexpr int OFF_H = 0;
constexpr int QS = 272;
constexpr int OFF_Q = 75776;
constexpr int SLOT3 = 18432;
constexpr int OFF_B1 = 147456;
constexpr int LDS1 = 148608;

DEV bf16x8 tr_frag(const char* p0, const char* p1) {
  s16x4 a = __builtin_amdgcn_ds_read_tr16_b64_v4i16((lds_s16x4_ptr)p0);
  s16x4 b = __builtin_amdgcn_ds_read_tr16_b64_v4i16((lds_s16x4_ptr)p1);
  bf16x8 r;
  r[0] = a[0]; r[1] = a[1]; r[2] = a[2]; r[3] = a[3];
  r[4] = b[0]; r[5] = b[1]; r[6] = b[2]; r[7] = b[3];
  return r;
}
DEV void glds16(const void* sbase, unsigned voff, unsigned lds_dst) {
  unsigned keep;
  asm volatile("s_mov_b32 %0, m0\n\ts_mov_b32 m0, %3\n\ts_nop 0\n\tglobal_load_lds_dwordx4 %1, %2\n\ts_mov_b32 m0, %0"
               : "=&s"(keep) : "v"(voff), "s"(sbase), "s"(lds_dst) : "memory");
}
#define VMWAIT(n) asm volatile("s_waitcnt vmcnt(" #n ")" ::: "memory")
#define BARRIER() do { asm volatile("s_waitcnt lgkmcnt(0)" ::: "memory"); __builtin_amdgcn_s_barrier(); asm volatile("" ::: "memory"); } while (0)

DEV float gelu_tanh(float v) {
  float a = v * v * (0.044715f * 2.f * 0.7978845608028654f) + 2.f * 0.7978845608028654f;
  return v * __builtin_amdgcn_rcpf(1.f + __expf(-a * v));
}

__global__ __launch_bounds__(64) void k0(const unsigned short* __restrict__ x, unsigned short* __restrict__ xn) {
  const int lane = threadIdx.x & 63, l8 = lane & 7;
  const int row = blockIdx.x * 8 + (threadIdx.x >> 3);
  const unsigned short* xr = x + (size_t)row * DM;
  u32x4 v[9];
#pragma unroll
  for (int i = 0; i < 9; ++i) v[i] = *(const u32x4*)(xr + (l8 + 8 * i) * 8);
  float s = 0.f;
#pragma unroll
  for (int i = 0; i < 9; ++i)
#pragma unroll
    for (int j = 0; j < 4; ++j) s += bf2f(v[i][j] & 0xffffu) + bf2f(v[i][j] >> 16);
  s += __shfl_xor(s, 1); s += __shfl_xor(s, 2); s += __shfl_xor(s, 4);
  float mean = s * (1.f / 576.f);
  float ss = 0.f;
#pragma unroll
  for (int i = 0; i < 9; ++i)
#pragma unroll
    for (int j = 0; j < 4; ++j) {
      float a = bf2f(v[i][j] & 0xffffu) - mean, b = bf2f(v[i][j] >> 16) - mean;
      ss += a * a + b * b;
    }
  ss += __shfl_xor(ss, 1); ss += __shfl_xor(ss, 2); ss += __shfl_xor(ss, 4);
  float var = rbf(ss * (1.f / 576.f));
  float den = rbf(sqrtf(rbf(var + 1e-5f)));
  float mb = rbf(mean);
#pragma unroll
  for (int i = 0; i < 9; ++i) {
    u32x4 o;
#pragma unroll
    for (int j = 0; j < 4; ++j) {
      float a = rbf(bf2f(v[i][j] & 0xffffu) - mb) / den;
      float c = rbf(bf2f(v[i][j] >> 16) - mb) / den;
      o[j] = f2bf(a) | (f2bf(c) << 16);
    }
    *(u32x4*)(xn + (size_t)row * DM + (l8 + 8 * i) * 8) = o;
  }
}

__global__ __launch_bounds__(512, 2) void k1(const unsigned short* __restrict__ xn,
                                             const unsigned short* __restrict__ wqkv,
                                             const unsigned short* __restrict__ w1,
                                             const unsigned short* __restrict__ b1,
                                             const unsigned short* __restrict__ w2,
                                             unsigned short* __restrict__ o_ws,
                                             unsigned short* __restrict__ part) {
  extern __shared__ __attribute__((aligned(16))) char lds[];
  const int tid = threadIdx.x, lane = tid & 63, wave = __builtin_amdgcn_readfirstlane(tid >> 6);
  const int bid = blockIdx.x;
  const int mt = (bid >> 3) & 15;
  const int fs = (bid & 7) * 2 + (bid >> 7);
  const int r0 = mt * 128;
  const int wm = wave >> 1, wn = wave & 1;
  const int g = lane >> 4, l15 = lane & 15, q = l15 >> 2, p = lane & 3;
  const unsigned ldsb = (unsigned)(size_t)lds + (unsigned)wave * 1024u;
#define STAMP(i)
  if (tid < 288) ((float*)(lds + OFF_B1))[tid] = bf2f(b1[fs * FC + tid]);

  unsigned so[5];
  {
    auto w1off = [&](int pc) { int L = 64 * pc + lane, row = L / 36, c = L % 36; c ^= 2 * ((row >> 3) & 1); return (unsigned)((row * FF + fs * FC + c * 8) * 2); };
    auto wqoff = [&](int pc) { int L = 64 * pc + lane, row = L >> 4, c = L & 15; c ^= 2 * ((row & 3) | ((row >> 3) & 1) << 2);
                               int sg = c / 5, c5 = c % 5; if (sg > 2) { sg = 2; c5 = 0; } return (unsigned)((row * NQ + sg * 576 + fs * 36 + c5 * 8) * 2); };
    auto xoff = [&](int pc) { int L = 64 * pc + lane, row = L >> 2, c = (L & 3) ^ ((row >> 2) & 3);
                              int gr = r0 - 16 + row; gr = gr < 0 ? 0 : gr; return (unsigned)((gr * DM + c * 8) * 2); };
    so[0] = w1off(wave); so[1] = w1off(wave + 8);
    so[2] = wave < 2 ? w1off(16 + wave) : wqoff(wave - 2);
    so[3] = wave < 2 ? wqoff(6 + wave) : xoff(wave - 2);
    so[4] = wave < 3 ? xoff(6 + wave) : xoff(wave - 3);
  }
  auto stage1 = [&](int s) {
    const unsigned sb = (unsigned)(size_t)lds + (s & 3) * SLOT1;
    const char* w1s = (const char*)w1 + (size_t)s * 32 * FF * 2;
    const char* wqs = (const char*)wqkv + (size_t)s * 32 * NQ * 2;
    const char* xs = (const char*)xn + s * 64;
    glds16(w1s, so[0], sb + wave * 1024);
    glds16(w1s, so[1], sb + (wave + 8) * 1024);
    if (wave < 2) glds16(w1s, so[2], sb + (16 + wave) * 1024);
    else glds16(wqs, so[2], sb + SW1 + (wave - 2) * 1024);
    if (wave < 2) { unsigned o3 = so[3]; if (s == 17 && o3 > (32 * NQ - 8) * 2) o3 = (32 * NQ - 8) * 2; glds16(wqs, o3, sb + SW1 + (6 + wave) * 1024); }
    else glds16(xs, so[3], sb + SW1 + SWQ + (wave - 2) * 1024);
    glds16(xs, so[4], sb + SW1 + SWQ + (wave < 3 ? 6 + wave : wave - 3) * 1024);
  };

  f32x4 acc[2][13], acch = f32x4{0.f, 0.f, 0.f, 0.f};
#pragma unroll
  for (int r = 0; r < 2; ++r)
#pragma unroll
    for (int i = 0; i < 13; ++i) acc[r][i] = f32x4{0.f, 0.f, 0.f, 0.f};

  stage1(0); stage1(1); stage1(2);
  const int htile = 2 + (wave < 5 ? wave : 5);
  const bool typeB = wave >= 4;
  bf16x8 wf[14], xf0, xf1, xh;
#pragma unroll
  for (int i = 0; i < 14; ++i) wf[i] = bf16x8{0, 0, 0, 0, 0, 0, 0, 0};
  xf0 = xf1 = xh = wf[0];
  auto p1_read = [&](int s) {
    const char* sl = lds + (s & 3) * SLOT1;
    const char* xb = sl + SW1 + SWQ + (16 + 32 * wm + l15) * 64 + ((g ^ (l15 >> 2)) * 16);
    const char* wbase = sl + (8 * g + q) * 576 + 8 * p;
    const int dsw = wn ? -32 * (g & 1) : 32 * (g & 1);
    const char* wE = wbase + wn * 288 + dsw;
    const char* wO = wbase + wn * 288 - dsw;
    const char* qbase = sl + SW1 + (8 * g + q) * 256 + 8 * p;
    const int qsw = (q | ((g & 1) << 2));
#define WFRAG(nt) tr_frag(((nt) & 1 ? wO : wE) + 32 * (nt), ((nt) & 1 ? wO : wE) + 32 * (nt) + 4 * 576)
#define QFRAG(nt) tr_frag(qbase + 32 * ((nt) ^ qsw), qbase + 32 * ((nt) ^ qsw) + 4 * 256)
    xf0 = *(const bf16x8*)(xb);
    xf1 = *(const bf16x8*)(xb + 16 * 64);
#pragma unroll
    for (int i = 0; i < 9; ++i) wf[i] = WFRAG(i);
#pragma unroll
    for (int i = 0; i < 4; ++i) wf[9 + i] = QFRAG(wn * 4 + i);
    wf[13] = QFRAG(htile);
    xh = *(const bf16x8*)(xb - (16 + 32 * wm) * 64);
  };
  auto p1_mma = [&]() {
#pragma unroll
    for (int i = 0; i < 13; ++i) {
      acc[0][i] = __builtin_amdgcn_mfma_f32_16x16x32_bf16(wf[i], xf0, acc[0][i], 0, 0, 0);
      acc[1][i] = __builtin_amdgcn_mfma_f32_16x16x32_bf16(wf[i], xf1, acc[1][i], 0, 0, 0);
    }
    acch = __builtin_amdgcn_mfma_f32_16x16x32_bf16(wf[13], xh, acch, 0, 0, 0);
  };
  auto p1_sync = [&](int s) {
    VMWAIT(10);
    BARRIER();
    stage1(s + 3 < 18 ? s + 3 : 17);
  };
  p1_sync(0);
#pragma unroll 1
  for (int s = 0; s < 18; ++s) {
    p1_read(s);
    if (typeB) p1_sync(s + 1);
    p1_mma();
    if (!typeB) p1_sync(s + 1);
  }
  VMWAIT(0);
  BARRIER();

  STAMP(1)
  {
    char* hb = lds + OFF_H; char* qb = lds + OFF_Q;
#pragma unroll
    for (int i = 0; i < 9; ++i) {
      int f = (wn * 9 + i) * 16 + 4 * g;
      f32x4 bv = *(const f32x4*)(lds + OFF_B1 + f * 4);
#pragma unroll
      for (int r = 0; r < 2; ++r) {
        int m = 32 * wm + 16 * r + l15;
        u32x2 o;
#pragma unroll
        for (int j = 0; j < 2; ++j) {
          f32x2 v = unpk(pk_bf16(acc[r][i][2 * j], acc[r][i][2 * j + 1]));
          f32x2 bb; bb[0] = bv[2 * j]; bb[1] = bv[2 * j + 1];
          v = v + bb;
          v = unpk(pk_bf16(v[0], v[1]));
          f32x2 e = v * (v * v * (-0.044715f * 2.f * 0.7978845608028654f * 1.4426950408889634f) + (-2.f * 0.7978845608028654f * 1.4426950408889634f));
          f32x2 d; d[0] = __builtin_amdgcn_rcpf(1.f + __builtin_amdgcn_exp2f(e[0])); d[1] = __builtin_amdgcn_rcpf(1.f + __builtin_amdgcn_exp2f(e[1]));
          v = v * d;
          o[j] = pk_bf16(v[0], v[1]);
        }
        *(u32x2*)(hb + m * HS + f * 2) = o;
      }
    }
#pragma unroll
    for (int i = 9; i < 13; ++i)
#pragma unroll
      for (int r = 0; r < 2; ++r) {
        int col = wn * 64 + (i - 9) * 16 + 4 * g, row = 16 + 32 * wm + 16 * r + l15;
        u32x2 o; o[0] = pk_bf16(acc[r][i][0], acc[r][i][1]); o[1] = pk_bf16(acc[r][i][2], acc[r][i][3]);
        *(u32x2*)(qb + row * QS + col * 2) = o;
      }
    if (wave < 6) {
      int col = 32 + 16 * wave + 4 * g, row = l15;
      u32x2 o; o[0] = pk_bf16(acch[0], acch[1]); o[1] = pk_bf16(acch[2], acch[3]);
      *(u32x2*)(qb + row * QS + col * 2) = o;
    }
  }
  unsigned vo[3];
#pragma unroll
  for (int i = 0; i < 3; ++i) {
    int pc = wave + 8 * i; if (pc >= 18) pc -= 6;
    int L = 64 * pc + lane, row = L / 36, c = L % 36; c ^= 2 * ((row >> 3) & 1);
    vo[i] = (unsigned)(((fs * FC + row) * DM + c * 8) * 2);
  }
  auto stage3 = [&](int s) {
    const int nh = s >= 9, ks = s - 9 * nh;
    const unsigned sb = (unsigned)(size_t)lds + (s & 7) * SLOT3;
    const char* w2s = (const char*)w2 + (size_t)ks * 32 * DM * 2 + nh * 576;
    glds16(w2s, vo[0], sb + wave * 1024);
    glds16(w2s, vo[1], sb + (wave + 8) * 1024);
    glds16(w2s, vo[2], sb + (wave < 2 ? wave + 16 : wave + 10) * 1024);
  };
  BARRIER();
  STAMP(2)

  bf16x8 hf[2][9];
  {
    const char* hb = lds + OFF_H;
#pragma unroll
    for (int ks = 0; ks < 9; ++ks) {
      hf[0][ks] = *(const bf16x8*)(hb + (32 * wm + l15) * HS + ks * 64 + g * 16);
      hf[1][ks] = *(const bf16x8*)(hb + (32 * wm + 16 + l15) * HS + ks * 64 + g * 16);
    }
  }
  BARRIER();
#pragma unroll
  for (int i = 0; i < 4; ++i) stage3(i);
  {
    const char* qb = lds + OFF_Q;
    const int row = tid >> 2, hh = (tid >> 1) & 1, h2 = tid & 1;
    const unsigned* qp = (const unsigned*)(qb + (16 + row) * QS + 36 * hh);
    float qv[18];
#pragma unroll
    for (int d = 0; d < 9; ++d) { unsigned u = qp[d]; qv[2 * d] = bf2f(u & 0xffffu); qv[2 * d + 1] = bf2f(u >> 16); }
    float lg[4];
#pragma unroll
    for (int wi = 0; wi < 4; ++wi) {
      const int w = 4 * h2 + wi;
      const unsigned* kp = (const unsigned*)(qb + (16 + row - 2 * w) * QS + 80 + 36 * hh);
      float a = 0.f;
#pragma unroll
      for (int d = 0; d < 9; ++d) { unsigned u = kp[d]; a += qv[2 * d] * bf2f(u & 0xffffu); a += qv[2 * d + 1] * bf2f(u >> 16); }
      a = rbf(rbf(a) * 0.23570226039551584f);
      lg[wi] = (r0 + row - 2 * w >= 0) ? a : -1e9f;
    }
    float mx = fmaxf(fmaxf(lg[0], lg[1]), fmaxf(lg[2], lg[3]));
    mx = fmaxf(mx, __shfl_xor(mx, 1));
    float sum = 0.f;
#pragma unroll
    for (int wi = 0; wi < 4; ++wi) { lg[wi] = __expf(lg[wi] - mx); sum += lg[wi]; }
    sum += __shfl_xor(sum, 1);
    float rs = 1.f / sum;
    float ov[18];
#pragma unroll
    for (int d = 0; d < 18; ++d) ov[d] = 0.f;
#pragma unroll
    for (int wi = 0; wi < 4; ++wi) {
      const int w = 4 * h2 + wi;
      float pw = rbf(lg[wi] * rs);
      const unsigned* vp = (const unsigned*)(qb + (16 + row - 2 * w) * QS + 160 + 36 * hh);
#pragma unroll
      for (int d = 0; d < 9; ++d) { unsigned u = vp[d]; ov[2 * d] += pw * bf2f(u & 0xffffu); ov[2 * d + 1] += pw * bf2f(u >> 16); }
    }
#pragma unroll
    for (int d = 0; d < 18; ++d) ov[d] += __shfl_xor(ov[d], 1);
    unsigned* op = (unsigned*)(o_ws + (size_t)(r0 + row) * DM + fs * 36 + 18 * hh);
    if (h2 == 0) {
#pragma unroll
      for (int d = 0; d < 5; ++d) op[d] = pk_bf16(ov[2 * d], ov[2 * d + 1]);
    } else {
#pragma unroll
      for (int d = 5; d < 9; ++d) op[d] = pk_bf16(ov[2 * d], ov[2 * d + 1]);
    }
  }
  BARRIER();
  STAMP(3)
#pragma unroll
  for (int i = 4; i < 8; ++i) stage3(i);

  f32x4 acc2[2][9];
  bf16x8 vf[9];
#pragma unroll
  for (int i = 0; i < 9; ++i) vf[i] = bf16x8{0, 0, 0, 0, 0, 0, 0, 0};
  auto p3_read = [&](int s) {
    const char* sl = lds + (s & 7) * SLOT3;
    const char* wbase = sl + (8 * g + q) * 576 + 8 * p + wn * 288;
    const int dsw = wn ? -32 * (g & 1) : 32 * (g & 1);
    const char* wE = wbase + dsw;
    const char* wO = wbase - dsw;
#pragma unroll
    for (int i = 0; i < 9; ++i) {
      const char* a0 = ((i & 1) ? wO : wE) + 32 * i;
      vf[i] = tr_frag(a0, a0 + 4 * 576);
    }
  };
#define P3_MMA(ks) _Pragma("unroll") for (int i = 0; i < 9; ++i) { \
          acc2[0][i] = __builtin_amdgcn_mfma_f32_16x16x32_bf16(vf[i], hf[0][ks], acc2[0][i], 0, 0, 0); \
          acc2[1][i] = __builtin_amdgcn_mfma_f32_16x16x32_bf16(vf[i], hf[1][ks], acc2[1][i], 0, 0, 0); }
  auto p3_sync = [&](int s) {
    VMWAIT(12);
    BARRIER();
    stage3(s + 6 < 18 ? s + 6 : 17);
    stage3(s + 7 < 18 ? s + 7 : 17);
  };
  VMWAIT(18);
  BARRIER();
#pragma unroll 1
  for (int nh = 0; nh < 2; ++nh) {
#pragma unroll
    for (int r = 0; r < 2; ++r)
#pragma unroll
      for (int i = 0; i < 9; ++i) acc2[r][i] = f32x4{0.f, 0.f, 0.f, 0.f};
#pragma unroll
    for (int ks = 0; ks < 9; ++ks) {
      const int s = nh * 9 + ks;
      p3_read(s);
      if (typeB && (s & 1)) p3_sync(s + 1);
      P3_MMA(ks)
      if (!typeB && (s & 1)) p3_sync(s + 1);
    }
#pragma unroll
    for (int r = 0; r < 2; ++r)
#pragma unroll
      for (int i = 0; i < 9; ++i) {
        int n = nh * 288 + (9 * wn + i) * 16 + 4 * g, m = 32 * wm + 16 * r + l15;
        u32x2 o; o[0] = pk_bf16(acc2[r][i][0], acc2[r][i][1]); o[1] = pk_bf16(acc2[r][i][2], acc2[r][i][3]);
        *(u32x2*)(part + ((size_t)fs * 2048 + r0 + m) * DM + n) = o;
      }
  }
}

constexpr int W3ROW = 288, W3SLOT = 32 * W3ROW;
__global__ __launch_bounds__(256) void k2(const unsigned short* __restrict__ x,
                                          const unsigned short* __restrict__ wo,
                                          const unsigned short* __restrict__ b2,
                                          const unsigned short* __restrict__ o_ws,
                                          const unsigned short* __restrict__ part,
                                          unsigned short* __restrict__ out) {
  extern __shared__ __attribute__((aligned(16))) char lds[];
  const int tid = threadIdx.x, lane = tid & 63, wave = __builtin_amdgcn_readfirstlane(tid >> 6);
  const int row0 = (blockIdx.x >> 2) * 32, col0 = (blockIdx.x & 3) * 144;
  const int g = lane >> 4, l15 = lane & 15, q = l15 >> 2, p = lane & 3;
  const int r = wave & 1, nh = wave >> 1;
  unsigned so[3];
#pragma unroll
  for (int i = 0; i < 3; ++i) {
    int pc = wave + 4 * i; if (pc >= 9) pc -= 3;
    int L = 64 * pc + lane, row = L / 18, c = L % 18;
    so[i] = (unsigned)((row * DM + col0 + c * 8) * 2);
  }
  auto stage = [&](int s) {
    const unsigned sb = (unsigned)(size_t)lds + (s % 14) * W3SLOT;
    const char* ws = (const char*)wo + (size_t)s * 32 * DM * 2;
    glds16(ws, so[0], sb + wave * 1024);
    glds16(ws, so[1], sb + (wave + 4) * 1024);
    glds16(ws, so[2], sb + (wave == 0 ? 8 : wave + 5) * 1024);
  };
#pragma unroll
  for (int s = 0; s < 14; ++s) stage(s);
  const unsigned short* orow = o_ws + (size_t)(row0 + 16 * r + l15) * DM + 8 * g;
  bf16x8 of[18];
#pragma unroll
  for (int s = 0; s < 18; ++s) of[s] = *(const bf16x8*)(orow + s * 32);
  float* gs = (float*)(lds + 14 * W3SLOT);
#pragma unroll
  for (int j = 0; j < 3; ++j) {
    int c = tid + 256 * j;
    if (c < 576) {
      int rr = c / 18, cc = c % 18;
      const unsigned short* pp = part + (size_t)(row0 + rr) * DM + col0 + cc * 8;
      u32x4 t[16];
#pragma unroll
      for (int f = 0; f < 16; ++f) t[f] = *(const u32x4*)(pp + (size_t)f * 2048 * DM);
      u32x4 bb = *(const u32x4*)(b2 + col0 + cc * 8);
      float yv[8];
#pragma unroll
      for (int e = 0; e < 8; ++e) yv[e] = 0.f;
#pragma unroll
      for (int f = 0; f < 16; ++f)
#pragma unroll
        for (int e = 0; e < 4; ++e) { yv[2 * e] += bf2f(t[f][e] & 0xffffu); yv[2 * e + 1] += bf2f(t[f][e] >> 16); }
      f32x4 g0, g1;
#pragma unroll
      for (int e = 0; e < 4; ++e) {
        float y0 = rbf(rbf(yv[2 * e]) + bf2f(bb[e] & 0xffffu)), y1 = rbf(rbf(yv[2 * e + 1]) + bf2f(bb[e] >> 16));
        float a0 = rbf(y0 / (1.f + __expf(-y0))), a1 = rbf(y1 / (1.f + __expf(-y1)));
        if (e < 2) { g0[2 * e] = a0; g0[2 * e + 1] = a1; } else { g1[2 * e - 4] = a0; g1[2 * e - 3] = a1; }
      }
      *(f32x4*)(gs + rr * 148 + cc * 8) = g0; *(f32x4*)(gs + rr * 148 + cc * 8 + 4) = g1;
    }
  }
  f32x4 acc[5];
#pragma unroll
  for (int i = 0; i < 5; ++i) acc[i] = f32x4{0.f, 0.f, 0.f, 0.f};
  VMWAIT(0);
  BARRIER();
#pragma unroll
  for (int s = 0; s < 18; ++s) {
    if (s == 14) { VMWAIT(0); BARRIER(); }
    const char* sl = lds + (s % 14) * W3SLOT;
    const char* wrow0 = sl + (8 * g + q) * W3ROW + 8 * p;
#pragma unroll
    for (int i = 0; i < 5; ++i) {
      if (i < 4 || nh == 0) {
        const char* a0 = wrow0 + 32 * (nh * 5 + i);
        bf16x8 wf = tr_frag(a0, a0 + 4 * W3ROW);
        acc[i] = __builtin_amdgcn_mfma_f32_16x16x32_bf16(wf, of[s], acc[i], 0, 0, 0);
      }
    }
    if (s == 3) { BARRIER(); stage(14); stage(15); stage(16); stage(17); }
  }
#pragma unroll
  for (int i = 0; i < 5; ++i) {
    if (i < 4 || nh == 0) {
      int n = col0 + (nh * 5 + i) * 16 + 4 * g, m = row0 + 16 * r + l15;
      u32x2 xx = *(const u32x2*)(x + (size_t)m * DM + n);
      f32x4 gg = *(const f32x4*)(gs + (16 * r + l15) * 148 + (nh * 5 + i) * 16 + 4 * g);
      float xv[4] = {bf2f(xx[0] & 0xffffu), bf2f(xx[0] >> 16), bf2f(xx[1] & 0xffffu), bf2f(xx[1] >> 16)};
      unsigned ov[4];
#pragma unroll
      for (int j = 0; j < 4; ++j) ov[j] = f2bf(xv[j] + rbf(rbf(acc[i][j]) * gg[j]));
      u32x2 o; o[0] = ov[0] | (ov[1] << 16); o[1] = ov[2] | (ov[3] << 16);
      *(u32x2*)(out + (size_t)m * DM + n) = o;
    }
  }
}

extern "C" void kernel_launch(void* const* d_in, const int* in_sizes, int n_in,
                              void* d_out, int out_size, void* d_ws, size_t ws_size,
                              hipStream_t stream) {
  const unsigned short* x = (const unsigned short*)d_in[0];
  const unsigned short* wqkv = (const unsigned short*)d_in[1];
  const unsigned short* wo = (const unsigned short*)d_in[2];
  const unsigned short* w1 = (const unsigned short*)d_in[4];
  const unsigned short* b1 = (const unsigned short*)d_in[5];
  const unsigned short* w2 = (const unsigned short*)d_in[6];
  const unsigned short* b2 = (const unsigned short*)d_in[7];
  unsigned short* out = (unsigned short*)d_out;
  unsigned short* o_ws = (unsigned short*)d_ws;
  unsigned short* xn = (unsigned short*)((char*)d_ws + (200u << 20));
  unsigned short* part = (unsigned short*)((char*)d_ws + (4u << 20));
  static bool attr = false;
  if (!attr) { hipFuncSetAttribute((const void*)k1, hipFuncAttributeMaxDynamicSharedMemorySize, LDS1);
               hipFuncSetAttribute((const void*)k2, hipFuncAttributeMaxDynamicSharedMemorySize, 14 * W3SLOT + 32 * 148 * 4); attr = true; }
  k0<<<256, 64, 0, stream>>>(x, xn);
  k1<<<256, 512, LDS1, stream>>>(xn, wqkv, w1, b1, w2, o_ws, part);
  k2<<<256, 256, 14 * W3SLOT + 32 * 148 * 4, stream>>>(x, wo, b2, o_ws, part, out);
}
```

```cpp
#include <hip/hip_runtime.h>
#include <stdint.h>

using bf16x8 = __attribute__((ext_vector_type(8))) short;
using s16x4 = __attribute__((ext_vector_type(4))) short;
using f32x4 = __attribute__((ext_vector_type(4))) float;
using u32x4 = __attribute__((ext_vector_type(4))) unsigned;
using u32x2 = __attribute__((ext_vector_type(2))) unsigned;
typedef __attribute__((address_space(3))) s16x4* lds_s16x4_ptr;

#define DEV __device__ __forceinline__
DEV float bf2f(unsigned u) { return __uint_as_float(u << 16); }
DEV unsigned f2bf(float f) {
  unsigned u = __float_as_uint(f);
  u += 0x7FFFu + ((u >> 16) & 1u);
  return u >> 16;
}
DEV float rbf(float f) { return bf2f(f2bf(f)); }
using bf16x2_t = __attribute__((ext_vector_type(2))) __bf16;
DEV unsigned pk_bf16(float lo, float hi) { bf16x2_t r; r[0] = (__bf16)lo; r[1] = (__bf16)hi; return __builtin_bit_cast(unsigned, r); }
using f32x2 = __attribute__((ext_vector_type(2))) float;
DEV f32x2 unpk(unsigned u) { f32x2 r; r[0] = __uint_as_float(u << 16); r[1] = __uint_as_float(u & 0xffff0000u); return r; }

constexpr int DM = 576, FF = 4608, NQ = 1728;
constexpr int FC = 288;
constexpr int SW1 = 18432, SWQ = 8192, SX = 9216, SLOT1 = SW1 + SWQ + SX;
constexpr int HS = 592;
constexpr int OFF_H = 0;
constexpr int QS = 272;
constexpr int OFF_Q = 75776;
constexpr int SLOT3 = 18432;
constexpr int OFF_B1 = 147456;
constexpr int LDS1 = 148608;

DEV bf16x8 tr_frag(const char* p0, const char* p1) {
  s16x4 a = __builtin_amdgcn_ds_read_tr16_b64_v4i16((lds_s16x4_ptr)p0);
  s16x4 b = __builtin_amdgcn_ds_read_tr16_b64_v4i16((lds_s16x4_ptr)p1);
  bf16x8 r;
  r[0] = a[0]; r[1] = a[1]; r[2] = a[2]; r[3] = a[3];
  r[4] = b[0]; r[5] = b[1]; r[6] = b[2]; r[7] = b[3];
  return r;
}
DEV void glds16(const void* sbase, unsigned voff, unsigned lds_dst) {
  unsigned keep;
  asm volatile("s_mov_b32 %0, m0\n\ts_mov_b32 m0, %3\n\ts_nop 0\n\tglobal_load_lds_dwordx4 %1, %2\n\ts_mov_b32 m0, %0"
               : "=&s"(keep) : "v"(voff), "s"(sbase), "s"(lds_dst) : "memory");
}
#define VMWAIT(n) asm volatile("s_waitcnt vmcnt(" #n ")" ::: "memory")
#define BARRIER() do { asm volatile("s_waitcnt lgkmcnt(0)" ::: "memory"); __builtin_amdgcn_s_barrier(); asm volatile("" ::: "memory"); } while (0)

DEV float gelu_tanh(float v) {
  float a = v * v * (0.044715f * 2.f * 0.7978845608028654f) + 2.f * 0.7978845608028654f;
  return v * __builtin_amdgcn_rcpf(1.f + __expf(-a * v));
}

__global__ __launch_bounds__(64) void k0(const unsigned short* __restrict__ x, unsigned short* __restrict__ xn,
                                         const unsigned* __restrict__ pw1, const unsigned* __restrict__ pw2,
                                         const unsigned* __restrict__ pwq, const unsigned* __restrict__ pwo) {
  if (blockIdx.x >= 256) {
    const int b = blockIdx.x - 256;
    const int nch = 576 * 4608 * 2 / 16;
    u32x4 a = u32x4{0u, 0u, 0u, 0u};
    for (int c = b * 64 + threadIdx.x; c < nch; c += 768 * 64) { a ^= *(const u32x4*)(pw1 + (size_t)c * 4); a ^= *(const u32x4*)(pw2 + (size_t)c * 4); }
    for (int c = b * 64 + threadIdx.x; c < 576 * 1728 * 2 / 16; c += 768 * 64) a ^= *(const u32x4*)(pwq + (size_t)c * 4);
    for (int c = b * 64 + threadIdx.x; c < 576 * 576 * 2 / 16; c += 768 * 64) a ^= *(const u32x4*)(pwo + (size_t)c * 4);
    if ((a[0] ^ a[1] ^ a[2] ^ a[3]) == 0x9e3779b9u) xn[2048 * 576 + threadIdx.x] = 0;
    return;
  }
  const int lane = threadIdx.x & 63, l8 = lane & 7;
  const int row = blockIdx.x * 8 + (threadIdx.x >> 3);
  const unsigned short* xr = x + (size_t)row * DM;
  u32x4 v[9];
#pragma unroll
  for (int i = 0; i < 9; ++i) v[i] = *(const u32x4*)(xr + (l8 + 8 * i) * 8);
  float s = 0.f;
#pragma unroll
  for (int i = 0; i < 9; ++i)
#pragma unroll
    for (int j = 0; j < 4; ++j) s += bf2f(v[i][j] & 0xffffu) + bf2f(v[i][j] >> 16);
  s += __shfl_xor(s, 1); s += __shfl_xor(s, 2); s += __shfl_xor(s, 4);
  float mean = s * (1.f / 576.f);
  float ss = 0.f;
#pragma unroll
  for (int i = 0; i < 9; ++i)
#pragma unroll
    for (int j = 0; j < 4; ++j) {
      float a = bf2f(v[i][j] & 0xffffu) - mean, b = bf2f(v[i][j] >> 16) - mean;
      ss += a * a + b * b;
    }
  ss += __shfl_xor(ss, 1); ss += __shfl_xor(ss, 2); ss += __shfl_xor(ss, 4);
  float var = rbf(ss * (1.f / 576.f));
  float den = rbf(sqrtf(rbf(var + 1e-5f)));
  float mb = rbf(mean);
#pragma unroll
  for (int i = 0; i < 9; ++i) {
    u32x4 o;
#pragma unroll
    for (int j = 0; j < 4; ++j) {
      float a = rbf(bf2f(v[i][j] & 0xffffu) - mb) / den;
      float c = rbf(bf2f(v[i][j] >> 16) - mb) / den;
      o[j] = f2bf(a) | (f2bf(c) << 16);
    }
    *(u32x4*)(xn + (size_t)row * DM + (l8 + 8 * i) * 8) = o;
  }
}

__global__ __launch_bounds__(512, 2) void k1(const unsigned short* __restrict__ xn,
                                             const unsigned short* __restrict__ wqkv,
                                             const unsigned short* __restrict__ w1,
                                             const unsigned short* __restrict__ b1,
                                             const unsigned short* __restrict__ w2,
                                             unsigned short* __restrict__ o_ws,
                                             unsigned short* __restrict__ part) {
  extern __shared__ __attribute__((aligned(16))) char lds[];
  const int tid = threadIdx.x, lane = tid & 63, wave = __builtin_amdgcn_readfirstlane(tid >> 6);
  const int bid = blockIdx.x;
  const int mt = (bid >> 3) & 15;
  const int fs = (bid & 7) * 2 + (bid >> 7);
  const int r0 = mt * 128;
  const int wm = wave >> 1, wn = wave & 1;
  const int g = lane >> 4, l15 = lane & 15, q = l15 >> 2, p = lane & 3;
  const unsigned ldsb = (unsigned)(size_t)lds + (unsigned)wave * 1024u;
#define STAMP(i)
  if (tid < 288) ((float*)(lds + OFF_B1))[tid] = bf2f(b1[fs * FC + tid]);

  unsigned so[5];
  {
    auto w1off = [&](int pc) { int L = 64 * pc + lane, row = L / 36, c = L % 36; c ^= 2 * ((row >> 3) & 1); return (unsigned)((row * FF + fs * FC + c * 8) * 2); };
    auto wqoff = [&](int pc) { int L = 64 * pc + lane, row = L >> 4, c = L & 15; c ^= 2 * ((row & 3) | ((row >> 3) & 1) << 2);
                               int sg = c / 5, c5 = c % 5; if (sg > 2) { sg = 2; c5 = 0; } return (unsigned)((row * NQ + sg * 576 + fs * 36 + c5 * 8) * 2); };
    auto xoff = [&](int pc) { int L = 64 * pc + lane, row = L >> 2, c = (L & 3) ^ ((row >> 2) & 3);
                              int gr = r0 - 16 + row; gr = gr < 0 ? 0 : gr; return (unsigned)((gr * DM + c * 8) * 2); };
    so[0] = w1off(wave); so[1] = w1off(wave + 8);
    so[2] = wave < 2 ? w1off(16 + wave) : wqoff(wave - 2);
    so[3] = wave < 2 ? wqoff(6 + wave) : xoff(wave - 2);
    so[4] = wave < 3 ? xoff(6 + wave) : xoff(wave - 3);
  }
  auto stage1 = [&](int s) {
    const unsigned sb = (unsigned)(size_t)lds + (s & 3) * SLOT1;
    const char* w1s = (const char*)w1 + (size_t)s * 32 * FF * 2;
    const char* wqs = (const char*)wqkv + (size_t)s * 32 * NQ * 2;
    const char* xs = (const char*)xn + s * 64;
    glds16(w1s, so[0], sb + wave * 1024);
    glds16(w1s, so[1], sb + (wave + 8) * 1024);
    if (wave < 2) glds16(w1s, so[2], sb + (16 + wave) * 1024);
    else glds16(wqs, so[2], sb + SW1 + (wave - 2) * 1024);
    if (wave < 2) {
      const bool oob = s == 17 && so[3] > (32 * NQ - 8) * 2;
      if (!oob) glds16(wqs, so[3], sb + SW1 + (6 + wave) * 1024);
      else { u32x2 v = *(const u32x2*)(wqs + so[3]); *(u32x2*)(lds + (s & 3) * SLOT1 + SW1 + (6 + wave) * 1024 + lane * 16) = v; }
    }
    else glds16(xs, so[3], sb + SW1 + SWQ + (wave - 2) * 1024);
    glds16(xs, so[4], sb + SW1 + SWQ + (wave < 3 ? 6 + wave : wave - 3) * 1024);
  };

  f32x4 acc[2][13], acch = f32x4{0.f, 0.f, 0.f, 0.f};
#pragma unroll
  for (int r = 0; r < 2; ++r)
#pragma unroll
    for (int i = 0; i < 13; ++i) acc[r][i] = f32x4{0.f, 0.f, 0.f, 0.f};

  stage1(0); stage1(1); stage1(2);
  const int htile = 2 + (wave < 5 ? wave : 5);
  bf16x8 wa[7], wb[7], xa[3], xb3[3];
  auto p1_read0 = [&](int s, bf16x8* xf) {
    const char* sl = lds + (s & 3) * SLOT1;
    const char* xb = sl + SW1 + SWQ + (16 + 32 * wm + l15) * 64 + ((g ^ (l15 >> 2)) * 16);
    const char* wbase = sl + (8 * g + q) * 576 + 8 * p;
    const int dsw = wn ? -32 * (g & 1) : 32 * (g & 1);
    const char* wE = wbase + wn * 288 + dsw;
    const char* wO = wbase + wn * 288 - dsw;
#define WFRAG(nt) tr_frag(((nt) & 1 ? wO : wE) + 32 * (nt), ((nt) & 1 ? wO : wE) + 32 * (nt) + 4 * 576)
    xf[0] = *(const bf16x8*)(xb);
    xf[1] = *(const bf16x8*)(xb + 16 * 64);
#pragma unroll
    for (int i = 0; i < 7; ++i) wa[i] = WFRAG(i);
    xf[2] = *(const bf16x8*)(xb - (16 + 32 * wm) * 64);
  };
  auto p1_read1 = [&](int s) {
    const char* sl = lds + (s & 3) * SLOT1;
    const char* wbase = sl + (8 * g + q) * 576 + 8 * p;
    const int dsw = wn ? -32 * (g & 1) : 32 * (g & 1);
    const char* wE = wbase + wn * 288 + dsw;
    const char* wO = wbase + wn * 288 - dsw;
    const char* qbase = sl + SW1 + (8 * g + q) * 256 + 8 * p;
    const int qsw = (q | ((g & 1) << 2));
#define QFRAG(nt) tr_frag(qbase + 32 * ((nt) ^ qsw), qbase + 32 * ((nt) ^ qsw) + 4 * 256)
    wb[0] = WFRAG(7); wb[1] = WFRAG(8);
#pragma unroll
    for (int i = 0; i < 4; ++i) wb[2 + i] = QFRAG(wn * 4 + i);
    wb[6] = QFRAG(htile);
  };
  auto p1_mma0 = [&](const bf16x8* xf) {
#pragma unroll
    for (int i = 0; i < 7; ++i) {
      acc[0][i] = __builtin_amdgcn_mfma_f32_16x16x32_bf16(wa[i], xf[0], acc[0][i], 0, 0, 0);
      acc[1][i] = __builtin_amdgcn_mfma_f32_16x16x32_bf16(wa[i], xf[1], acc[1][i], 0, 0, 0);
    }
  };
  auto p1_mma1 = [&](const bf16x8* xf) {
#pragma unroll
    for (int i = 0; i < 6; ++i) {
      acc[0][7 + i] = __builtin_amdgcn_mfma_f32_16x16x32_bf16(wb[i], xf[0], acc[0][7 + i], 0, 0, 0);
      acc[1][7 + i] = __builtin_amdgcn_mfma_f32_16x16x32_bf16(wb[i], xf[1], acc[1][7 + i], 0, 0, 0);
    }
    acch = __builtin_amdgcn_mfma_f32_16x16x32_bf16(wb[6], xf[2], acch, 0, 0, 0);
  };
  auto p1_sync = [&](int s) {
    VMWAIT(10);
    BARRIER();
    stage1(s + 3 < 18 ? s + 3 : 17);
  };
  p1_sync(0);
  p1_read0(0, xa);
#pragma unroll 1
  for (int s = 0; s < 18; s += 2) {
    p1_read1(s);
    __builtin_amdgcn_sched_barrier(0);
    p1_mma0(xa);
    p1_sync(s + 1);
    p1_read0(s + 1, xb3);
    __builtin_amdgcn_sched_barrier(0);
    p1_mma1(xa);
    p1_read1(s + 1);
    __builtin_amdgcn_sched_barrier(0);
    p1_mma0(xb3);
    p1_sync(s + 2);
    p1_read0(s + 2 < 18 ? s + 2 : 17, xa);
    __builtin_amdgcn_sched_barrier(0);
    p1_mma1(xb3);
  }
  VMWAIT(0);
  BARRIER();

  STAMP(1)
  {
    char* hb = lds + OFF_H; char* qb = lds + OFF_Q;
#pragma unroll
    for (int i = 0; i < 9; ++i) {
      int f = (wn * 9 + i) * 16 + 4 * g;
      f32x4 bv = *(const f32x4*)(lds + OFF_B1 + f * 4);
#pragma unroll
      for (int r = 0; r < 2; ++r) {
        int m = 32 * wm + 16 * r + l15;
        u32x2 o;
#pragma unroll
        for (int j = 0; j < 2; ++j) {
          f32x2 v = unpk(pk_bf16(acc[r][i][2 * j], acc[r][i][2 * j + 1]));
          f32x2 bb; bb[0] = bv[2 * j]; bb[1] = bv[2 * j + 1];
          v = v + bb;
          v = unpk(pk_bf16(v[0], v[1]));
          f32x2 e = v * (v * v * (-0.044715f * 2.f * 0.7978845608028654f * 1.4426950408889634f) + (-2.f * 0.7978845608028654f * 1.4426950408889634f));
          f32x2 d; d[0] = __builtin_amdgcn_rcpf(1.f + __builtin_amdgcn_exp2f(e[0])); d[1] = __builtin_amdgcn_rcpf(1.f + __builtin_amdgcn_exp2f(e[1]));
          v = v * d;
          o[j] = pk_bf16(v[0], v[1]);
        }
        *(u32x2*)(hb + m * HS + f * 2) = o;
      }
    }
#pragma unroll
    for (int i = 9; i < 13; ++i)
#pragma unroll
      for (int r = 0; r < 2; ++r) {
        int col = wn * 64 + (i - 9) * 16 + 4 * g, row = 16 + 32 * wm + 16 * r + l15;
        u32x2 o; o[0] = pk_bf16(acc[r][i][0], acc[r][i][1]); o[1] = pk_bf16(acc[r][i][2], acc[r][i][3]);
        *(u32x2*)(qb + row * QS + col * 2) = o;
      }
    if (wave < 6) {
      int col = 32 + 16 * wave + 4 * g, row = l15;
      u32x2 o; o[0] = pk_bf16(acch[0], acch[1]); o[1] = pk_bf16(acch[2], acch[3]);
      *(u32x2*)(qb + row * QS + col * 2) = o;
    }
  }
  unsigned vo[3];
#pragma unroll
  for (int i = 0; i < 3; ++i) {
    int pc = wave + 8 * i; if (pc >= 18) pc -= 6;
    int L = 64 * pc + lane, row = L / 36, c = L % 36; c ^= 2 * ((row >> 3) & 1);
    vo[i] = (unsigned)(((fs * FC + row) * DM + c * 8) * 2);
  }
  auto stage3 = [&](int s) {
    const int nh = s >= 9, ks = s - 9 * nh;
    const unsigned sb = (unsigned)(size_t)lds + (s & 7) * SLOT3;
    const char* w2s = (const char*)w2 + (size_t)ks * 32 * DM * 2 + nh * 576;
    glds16(w2s, vo[0], sb + wave * 1024);
    glds16(w2s, vo[1], sb + (wave + 8) * 1024);
    glds16(w2s, vo[2], sb + (wave < 2 ? wave + 16 : wave + 10) * 1024);
  };
  BARRIER();
  STAMP(2)

  bf16x8 hf[2][9];
  {
    const char* hb = lds + OFF_H;
#pragma unroll
    for (int ks = 0; ks < 9; ++ks) {
      hf[0][ks] = *(const bf16x8*)(hb + (32 * wm + l15) * HS + ks * 64 + g * 16);
      hf[1][ks] = *(const bf16x8*)(hb + (32 * wm + 16 + l15) * HS + ks * 64 + g * 16);
    }
  }
  BARRIER();
#pragma unroll
  for (int i = 0; i < 4; ++i) stage3(i);
  {
    const char* qb = lds + OFF_Q;
    const int row = tid >> 2, hh = (tid >> 1) & 1, h2 = tid & 1;
    const unsigned* qp = (const unsigned*)(qb + (16 + row) * QS + 36 * hh);
    float qv[18];
#pragma unroll
    for (int d = 0; d < 9; ++d) { unsigned u = qp[d]; qv[2 * d] = bf2f(u & 0xffffu); qv[2 * d + 1] = bf2f(u >> 16); }
    float lg[4];
#pragma unroll
    for (int wi = 0; wi < 4; ++wi) {
      const int w = 4 * h2 + wi;
      const unsigned* kp = (const unsigned*)(qb + (16 + row - 2 * w) * QS + 80 + 36 * hh);
      float a = 0.f;
#pragma unroll
      for (int d = 0; d < 9; ++d) { unsigned u = kp[d]; a += qv[2 * d] * bf2f(u & 0xffffu); a += qv[2 * d + 1] * bf2f(u >> 16); }
      a = rbf(rbf(a) * 0.23570226039551584f);
      lg[wi] = (r0 + row - 2 * w >= 0) ? a : -1e9f;
    }
    float mx = fmaxf(fmaxf(lg[0], lg[1]), fmaxf(lg[2], lg[3]));
    mx = fmaxf(mx, __shfl_xor(mx, 1));
    float sum = 0.f;
#pragma unroll
    for (int wi = 0; wi < 4; ++wi) { lg[wi] = __expf(lg[wi] - mx); sum += lg[wi]; }
    sum += __shfl_xor(sum, 1);
    float rs = 1.f / sum;
    float ov[18];
#pragma unroll
    for (int d = 0; d < 18; ++d) ov[d] = 0.f;
#pragma unroll
    for (int wi = 0; wi < 4; ++wi) {
      const int w = 4 * h2 + wi;
      float pw = rbf(lg[wi] * rs);
      const unsigned* vp = (const unsigned*)(qb + (16 + row - 2 * w) * QS + 160 + 36 * hh);
#pragma unroll
      for (int d = 0; d < 9; ++d) { unsigned u = vp[d]; ov[2 * d] += pw * bf2f(u & 0xffffu); ov[2 * d + 1] += pw * bf2f(u >> 16); }
    }
#pragma unroll
    for (int d = 0; d < 18; ++d) ov[d] += __shfl_xor(ov[d], 1);
    unsigned* op = (unsigned*)(o_ws + (size_t)(r0 + row) * DM + fs * 36 + 18 * hh);
    if (h2 == 0) {
#pragma unroll
      for (int d = 0; d < 5; ++d) op[d] = pk_bf16(ov[2 * d], ov[2 * d + 1]);
    } else {
#pragma unroll
      for (int d = 5; d < 9; ++d) op[d] = pk_bf16(ov[2 * d], ov[2 * d + 1]);
    }
  }
  BARRIER();
  STAMP(3)
#pragma unroll
  for (int i = 4; i < 8; ++i) stage3(i);

  f32x4 acc2[2][9];
  bf16x8 va[5], vb[4];
  auto p3_read0 = [&](int s) {
    const char* sl = lds + (s & 7) * SLOT3;
    const char* wbase = sl + (8 * g + q) * 576 + 8 * p + wn * 288;
    const int dsw = wn ? -32 * (g & 1) : 32 * (g & 1);
#pragma unroll
    for (int i = 0; i < 5; ++i) {
      const char* a0 = wbase + ((i & 1) ? -dsw : dsw) + 32 * i;
      va[i] = tr_frag(a0, a0 + 4 * 576);
    }
  };
  auto p3_read1 = [&](int s) {
    const char* sl = lds + (s & 7) * SLOT3;
    const char* wbase = sl + (8 * g + q) * 576 + 8 * p + wn * 288;
    const int dsw = wn ? -32 * (g & 1) : 32 * (g & 1);
#pragma unroll
    for (int i = 5; i < 9; ++i) {
      const char* a0 = wbase + ((i & 1) ? -dsw : dsw) + 32 * i;
      vb[i - 5] = tr_frag(a0, a0 + 4 * 576);
    }
  };
  auto p3_sync = [&](int s) {
    VMWAIT(18);
    BARRIER();
    stage3(s + 7 < 18 ? s + 7 : 17);
  };
  VMWAIT(21);
  BARRIER();
  p3_read0(0);
#pragma unroll 1
  for (int nh = 0; nh < 2; ++nh) {
#pragma unroll
    for (int r = 0; r < 2; ++r)
#pragma unroll
      for (int i = 0; i < 9; ++i) acc2[r][i] = f32x4{0.f, 0.f, 0.f, 0.f};
#pragma unroll
    for (int ks = 0; ks < 9; ++ks) {
      const int s = nh * 9 + ks;
      p3_read1(s);
      __builtin_amdgcn_sched_barrier(0);
#pragma unroll
      for (int i = 0; i < 5; ++i) {
        acc2[0][i] = __builtin_amdgcn_mfma_f32_16x16x32_bf16(va[i], hf[0][ks], acc2[0][i], 0, 0, 0);
        acc2[1][i] = __builtin_amdgcn_mfma_f32_16x16x32_bf16(va[i], hf[1][ks], acc2[1][i], 0, 0, 0);
      }
      p3_sync(s + 1);
      p3_read0(s + 1 < 18 ? s + 1 : 17);
      __builtin_amdgcn_sched_barrier(0);
#pragma unroll
      for (int i = 0; i < 4; ++i) {
        acc2[0][5 + i] = __builtin_amdgcn_mfma_f32_16x16x32_bf16(vb[i], hf[0][ks], acc2[0][5 + i], 0, 0, 0);
        acc2[1][5 + i] = __builtin_amdgcn_mfma_f32_16x16x32_bf16(vb[i], hf[1][ks], acc2[1][5 + i], 0, 0, 0);
      }
    }
#pragma unroll
    for (int r = 0; r < 2; ++r)
#pragma unroll
      for (int i = 0; i < 9; ++i) {
        int n = nh * 288 + (9 * wn + i) * 16 + 4 * g, m = 32 * wm + 16 * r + l15;
        u32x2 o; o[0] = pk_bf16(acc2[r][i][0], acc2[r][i][1]); o[1] = pk_bf16(acc2[r][i][2], acc2[r][i][3]);
        *(u32x2*)(part + ((size_t)fs * 2048 + r0 + m) * DM + n) = o;
      }
  }
}

constexpr int W3ROW = 288, W3SLOT = 32 * W3ROW;
__global__ __launch_bounds__(512) void k2(const unsigned short* __restrict__ x,
                                          const unsigned short* __restrict__ wo,
                                          const unsigned short* __restrict__ b2,
                                          const unsigned short* __restrict__ o_ws,
                                          const unsigned short* __restrict__ part,
                                          unsigned short* __restrict__ out) {
  extern __shared__ __attribute__((aligned(16))) char lds[];
  const int tid = threadIdx.x, lane = tid & 63, wave = __builtin_amdgcn_readfirstlane(tid >> 6);
  const int row0 = (blockIdx.x >> 2) * 32, col0 = (blockIdx.x & 3) * 144;
  const int g = lane >> 4, l15 = lane & 15, q = l15 >> 2, p = lane & 3;
  const int r = wave & 1, nh = (wave >> 1) & 1;
  unsigned so[3];
#pragma unroll
  for (int i = 0; i < 3; ++i) {
    int pc = wave + 4 * i; if (pc >= 9) pc -= 3;
    int L = 64 * pc + lane, row = L / 18, c = L % 18;
    so[i] = (unsigned)((row * DM + col0 + c * 8) * 2);
  }
  auto stage = [&](int s) {
    if (wave >= 4) return;
    const unsigned sb = (unsigned)(size_t)lds + (s % 14) * W3SLOT;
    const char* ws = (const char*)wo + (size_t)s * 32 * DM * 2;
    glds16(ws, so[0], sb + wave * 1024);
    glds16(ws, so[1], sb + (wave + 4) * 1024);
    glds16(ws, so[2], sb + (wave == 0 ? 8 : wave + 5) * 1024);
  };
#pragma unroll
  for (int s = 0; s < 14; ++s) stage(s);
  const unsigned short* orow = o_ws + (size_t)(row0 + 16 * r + l15) * DM + 8 * g;
  bf16x8 of[18];
  if (wave < 4) {
#pragma unroll
    for (int s = 0; s < 18; ++s) of[s] = *(const bf16x8*)(orow + s * 32);
  }
  float* gs = (float*)(lds + 14 * W3SLOT);
#pragma unroll
  for (int j = 0; j < 2; ++j) {
    int c = tid + 512 * j;
    if (c < 576) {
      int rr = c / 18, cc = c % 18;
      const unsigned short* pp = part + (size_t)(row0 + rr) * DM + col0 + cc * 8;
      u32x4 t[16];
#pragma unroll
      for (int f = 0; f < 16; ++f) t[f] = *(const u32x4*)(pp + (size_t)f * 2048 * DM);
      u32x4 bb = *(const u32x4*)(b2 + col0 + cc * 8);
      float yv[8];
#pragma unroll
      for (int e = 0; e < 8; ++e) yv[e] = 0.f;
#pragma unroll
      for (int f = 0; f < 16; ++f)
#pragma unroll
        for (int e = 0; e < 4; ++e) { yv[2 * e] += bf2f(t[f][e] & 0xffffu); yv[2 * e + 1] += bf2f(t[f][e] >> 16); }
      f32x4 g0, g1;
#pragma unroll
      for (int e = 0; e < 4; ++e) {
        float y0 = rbf(rbf(yv[2 * e]) + bf2f(bb[e] & 0xffffu)), y1 = rbf(rbf(yv[2 * e + 1]) + bf2f(bb[e] >> 16));
        float a0 = rbf(y0 / (1.f + __expf(-y0))), a1 = rbf(y1 / (1.f + __expf(-y1)));
        if (e < 2) { g0[2 * e] = a0; g0[2 * e + 1] = a1; } else { g1[2 * e - 4] = a0; g1[2 * e - 3] = a1; }
      }
      *(f32x4*)(gs + rr * 148 + cc * 8) = g0; *(f32x4*)(gs + rr * 148 + cc * 8 + 4) = g1;
    }
  }
  f32x4 acc[5];
#pragma unroll
  for (int i = 0; i < 5; ++i) acc[i] = f32x4{0.f, 0.f, 0.f, 0.f};
  VMWAIT(0);
  BARRIER();
  if (wave >= 4) { BARRIER(); BARRIER(); return; }
#pragma unroll
  for (int s = 0; s < 18; ++s) {
    if (s == 14) { VMWAIT(0); BARRIER(); }
    const char* sl = lds + (s % 14) * W3SLOT;
    const char* wrow0 = sl + (8 * g + q) * W3ROW + 8 * p;
#pragma unroll
    for (int i = 0; i < 5; ++i) {
      if (i < 4 || nh == 0) {
        const char* a0 = wrow0 + 32 * (nh * 5 + i);
        bf16x8 wf = tr_frag(a0, a0 + 4 * W3ROW);
        acc[i] = __builtin_amdgcn_mfma_f32_16x16x32_bf16(wf, of[s], acc[i], 0, 0, 0);
      }
    }
    if (s == 3) { BARRIER(); stage(14); stage(15); stage(16); stage(17); }
  }
#pragma unroll
  for (int i = 0; i < 5; ++i) {
    if (i < 4 || nh == 0) {
      int n = col0 + (nh * 5 + i) * 16 + 4 * g, m = row0 + 16 * r + l15;
      u32x2 xx = *(const u32x2*)(x + (size_t)m * DM + n);
      f32x4 gg = *(const f32x4*)(gs + (16 * r + l15) * 148 + (nh * 5 + i) * 16 + 4 * g);
      float xv[4] = {bf2f(xx[0] & 0xffffu), bf2f(xx[0] >> 16), bf2f(xx[1] & 0xffffu), bf2f(xx[1] >> 16)};
      unsigned ov[4];
#pragma unroll
      for (int j = 0; j < 4; ++j) ov[j] = f2bf(xv[j] + rbf(rbf(acc[i][j]) * gg[j]));
      u32x2 o; o[0] = ov[0] | (ov[1] << 16); o[1] = ov[2] | (ov[3] << 16);
      *(u32x2*)(out + (size_t)m * DM + n) = o;
    }
  }
}

extern "C" void kernel_launch(void* const* d_in, const int* in_sizes, int n_in,
                              void* d_out, int out_size, void* d_ws, size_t ws_size,
                              hipStream_t stream) {
  const unsigned short* x = (const unsigned short*)d_in[0];
  const unsigned short* wqkv = (const unsigned short*)d_in[1];
  const unsigned short* wo = (const unsigned short*)d_in[2];
  const unsigned short* w1 = (const unsigned short*)d_in[4];
  const unsigned short* b1 = (const unsigned short*)d_in[5];
  const unsigned short* w2 = (const unsigned short*)d_in[6];
  const unsigned short* b2 = (const unsigned short*)d_in[7];
  unsigned short* out = (unsigned short*)d_out;
  unsigned short* o_ws = (unsigned short*)d_ws;
  unsigned short* xn = (unsigned short*)((char*)d_ws + (200u << 20));
  unsigned short* part = (unsigned short*)((char*)d_ws + (4u << 20));
  static bool attr = false;
  if (!attr) { hipFuncSetAttribute((const void*)k1, hipFuncAttributeMaxDynamicSharedMemorySize, LDS1);
               hipFuncSetAttribute((const void*)k2, hipFuncAttributeMaxDynamicSharedMemorySize, 14 * W3SLOT + 32 * 148 * 4); attr = true; }
  k0<<<1024, 64, 0, stream>>>(x, xn, (const unsigned*)w1, (const unsigned*)w2, (const unsigned*)wqkv, (const unsigned*)wo);
  k1<<<256, 512, LDS1, stream>>>(xn, wqkv, w1, b1, w2, o_ws, part);
  k2<<<256, 512, 14 * W3SLOT + 32 * 148 * 4, stream>>>(x, wo, b2, o_ws, part, out);
}
```

```cpp
#include <hip/hip_runtime.h>
#include <stdint.h>

using bf16x8 = __attribute__((ext_vector_type(8))) short;
using s16x4 = __attribute__((ext_vector_type(4))) short;
using f32x4 = __attribute__((ext_vector_type(4))) float;
using u32x4 = __attribute__((ext_vector_type(4))) unsigned;
using u32x2 = __attribute__((ext_vector_type(2))) unsigned;
typedef __attribute__((address_space(3))) s16x4* lds_s16x4_ptr;

#define DEV __device__ __forceinline__
DEV float bf2f(unsigned u) { return __uint_as_float(u << 16); }
DEV unsigned f2bf(float f) {
  unsigned u = __float_as_uint(f);
  u += 0x7FFFu + ((u >> 16) & 1u);
  return u >> 16;
}
DEV float rbf(float f) { return bf2f(f2bf(f)); }
using bf16x2_t = __attribute__((ext_vector_type(2))) __bf16;
DEV unsigned pk_bf16(float lo, float hi) { bf16x2_t r; r[0] = (__bf16)lo; r[1] = (__bf16)hi; return __builtin_bit_cast(unsigned, r); }
using f32x2 = __attribute__((ext_vector_type(2))) float;
DEV f32x2 unpk(unsigned u) { f32x2 r; r[0] = __uint_as_float(u << 16); r[1] = __uint_as_float(u & 0xffff0000u); return r; }

constexpr int DM = 576, FF = 4608, NQ = 1728;
constexpr int FC = 288;
constexpr int SW1 = 18432, SWQ = 8192, SX = 9216, SLOT1 = SW1 + SWQ + SX;
constexpr int HS = 592;
constexpr int OFF_H = 0;
constexpr int QS = 272;
constexpr int OFF_Q = 75776;
constexpr int SLOT3 = 18432;
constexpr int OFF_B1 = 147456;
constexpr int LDS1 = 148608;

DEV bf16x8 tr_frag(const char* p0, const char* p1) {
  s16x4 a = __builtin_amdgcn_ds_read_tr16_b64_v4i16((lds_s16x4_ptr)p0);
  s16x4 b = __builtin_amdgcn_ds_read_tr16_b64_v4i16((lds_s16x4_ptr)p1);
  bf16x8 r;
  r[0] = a[0]; r[1] = a[1]; r[2] = a[2]; r[3] = a[3];
  r[4] = b[0]; r[5] = b[1]; r[6] = b[2]; r[7] = b[3];
  return r;
}
DEV void glds16(const void* sbase, unsigned voff, unsigned lds_dst) {
  unsigned keep;
  asm volatile("s_mov_b32 %0, m0\n\ts_mov_b32 m0, %3\n\ts_nop 0\n\tglobal_load_lds_dwordx4 %1, %2\n\ts_mov_b32 m0, %0"
               : "=&s"(keep) : "v"(voff), "s"(sbase), "s"(lds_dst) : "memory");
}
#define VMWAIT(n) asm volatile("s_waitcnt vmcnt(" #n ")" ::: "memory")
#define BARRIER() do { asm volatile("s_waitcnt lgkmcnt(0)" ::: "memory"); __builtin_amdgcn_s_barrier(); asm volatile("" ::: "memory"); } while (0)

DEV float gelu_tanh(float v) {
  float a = v * v * (0.044715f * 2.f * 0.7978845608028654f) + 2.f * 0.7978845608028654f;
  return v * __builtin_amdgcn_rcpf(1.f + __expf(-a * v));
}

__global__ __launch_bounds__(64) void k0(const unsigned short* __restrict__ x, unsigned short* __restrict__ xn,
                                         const unsigned* __restrict__ pw1, const unsigned* __restrict__ pw2,
                                         const unsigned* __restrict__ pwq, const unsigned* __restrict__ pwo) {
  if (blockIdx.x >= 256) {
    const int b = blockIdx.x - 256;
    const int nch = 576 * 4608 * 2 / 16;
    u32x4 a = u32x4{0u, 0u, 0u, 0u};
    for (int c = b * 64 + threadIdx.x; c < nch; c += 768 * 64) { a ^= *(const u32x4*)(pw1 + (size_t)c * 4); a ^= *(const u32x4*)(pw2 + (size_t)c * 4); }
    for (int c = b * 64 + threadIdx.x; c < 576 * 1728 * 2 / 16; c += 768 * 64) a ^= *(const u32x4*)(pwq + (size_t)c * 4);
    for (int c = b * 64 + threadIdx.x; c < 576 * 576 * 2 / 16; c += 768 * 64) a ^= *(const u32x4*)(pwo + (size_t)c * 4);
    if ((a[0] ^ a[1] ^ a[2] ^ a[3]) == 0x9e3779b9u) xn[2048 * 576 + threadIdx.x] = 0;
    return;
  }
  const int lane = threadIdx.x & 63, l8 = lane & 7;
  const int row = blockIdx.x * 8 + (threadIdx.x >> 3);
  const unsigned short* xr = x + (size_t)row * DM;
  u32x4 v[9];
#pragma unroll
  for (int i = 0; i < 9; ++i) v[i] = *(const u32x4*)(xr + (l8 + 8 * i) * 8);
  float s = 0.f;
#pragma unroll
  for (int i = 0; i < 9; ++i)
#pragma unroll
    for (int j = 0; j < 4; ++j) s += bf2f(v[i][j] & 0xffffu) + bf2f(v[i][j] >> 16);
  s += __shfl_xor(s, 1); s += __shfl_xor(s, 2); s += __shfl_xor(s, 4);
  float mean = s * (1.f / 576.f);
  float ss = 0.f;
#pragma unroll
  for (int i = 0; i < 9; ++i)
#pragma unroll
    for (int j = 0; j < 4; ++j) {
      float a = bf2f(v[i][j] & 0xffffu) - mean, b = bf2f(v[i][j] >> 16) - mean;
      ss += a * a + b * b;
    }
  ss += __shfl_xor(ss, 1); ss += __shfl_xor(ss, 2); ss += __shfl_xor(ss, 4);
  float var = rbf(ss * (1.f / 576.f));
  float den = rbf(sqrtf(rbf(var + 1e-5f)));
  float mb = rbf(mean);
#pragma unroll
  for (int i = 0; i < 9; ++i) {
    u32x4 o;
#pragma unroll
    for (int j = 0; j < 4; ++j) {
      float a = rbf(bf2f(v[i][j] & 0xffffu) - mb) / den;
      float c = rbf(bf2f(v[i][j] >> 16) - mb) / den;
      o[j] = f2bf(a) | (f2bf(c) << 16);
    }
    *(u32x4*)(xn + (size_t)row * DM + (l8 + 8 * i) * 8) = o;
  }
}

__global__ __launch_bounds__(512, 2) void k1(const unsigned short* __restrict__ xn,
                                             const unsigned short* __restrict__ wqkv,
                                             const unsigned short* __restrict__ w1,
                                             const unsigned short* __restrict__ b1,
                                             const unsigned short* __restrict__ w2,
                                             unsigned short* __restrict__ o_ws,
                                             unsigned short* __restrict__ part) {
  extern __shared__ __attribute__((aligned(16))) char lds[];
  const int tid = threadIdx.x, lane = tid & 63, wave = __builtin_amdgcn_readfirstlane(tid >> 6);
  const int bid = blockIdx.x;
  const int mt = (bid >> 3) & 15;
  const int fs = (bid & 7) * 2 + (bid >> 7);
  const int r0 = mt * 128;
  const int wm = wave >> 1, wn = wave & 1;
  const int g = lane >> 4, l15 = lane & 15, q = l15 >> 2, p = lane & 3;
  const unsigned ldsb = (unsigned)(size_t)lds + (unsigned)wave * 1024u;
  if (tid < 288) ((float*)(lds + OFF_B1))[tid] = bf2f(b1[fs * FC + tid]);

  unsigned so[5];
  {
    auto w1off = [&](int pc) { int L = 64 * pc + lane, row = L / 36, c = L % 36; c ^= 2 * ((row >> 3) & 1); return (unsigned)((row * FF + fs * FC + c * 8) * 2); };
    auto wqoff = [&](int pc) { int L = 64 * pc + lane, row = L >> 4, c = L & 15; c ^= 2 * ((row & 3) | ((row >> 3) & 1) << 2);
                               int sg = c / 5, c5 = c % 5; if (sg > 2) { sg = 2; c5 = 0; } return (unsigned)((row * NQ + sg * 576 + fs * 36 + c5 * 8) * 2); };
    auto xoff = [&](int pc) { int L = 64 * pc + lane, row = L >> 2, c = (L & 3) ^ ((row >> 2) & 3);
                              int gr = r0 - 16 + row; gr = gr < 0 ? 0 : gr; return (unsigned)((gr * DM + c * 8) * 2); };
    so[0] = w1off(wave); so[1] = w1off(wave + 8);
    so[2] = wave < 2 ? w1off(16 + wave) : wqoff(wave - 2);
    so[3] = wave < 2 ? wqoff(6 + wave) : xoff(wave - 2);
    so[4] = wave < 3 ? xoff(6 + wave) : xoff(wave - 3);
  }
  auto stage1 = [&](int s) {
    const unsigned sb = (unsigned)(size_t)lds + (s & 3) * SLOT1;
    const char* w1s = (const char*)w1 + (size_t)s * 32 * FF * 2;
    const char* wqs = (const char*)wqkv + (size_t)s * 32 * NQ * 2;
    const char* xs = (const char*)xn + s * 64;
    glds16(w1s, so[0], sb + wave * 1024);
    glds16(w1s, so[1], sb + (wave + 8) * 1024);
    if (wave < 2) glds16(w1s, so[2], sb + (16 + wave) * 1024);
    else glds16(wqs, so[2], sb + SW1 + (wave - 2) * 1024);
    if (wave < 2) {
      const bool oob = s == 17 && so[3] > (32 * NQ - 8) * 2;
      if (!oob) glds16(wqs, so[3], sb + SW1 + (6 + wave) * 1024);
      else { u32x2 v = *(const u32x2*)(wqs + so[3]); *(u32x2*)(lds + (s & 3) * SLOT1 + SW1 + (6 + wave) * 1024 + lane * 16) = v; }
    }
    else glds16(xs, so[3], sb + SW1 + SWQ + (wave - 2) * 1024);
    glds16(xs, so[4], sb + SW1 + SWQ + (wave < 3 ? 6 + wave : wave - 3) * 1024);
  };

  f32x4 acc[2][13], acch = f32x4{0.f, 0.f, 0.f, 0.f};
#pragma unroll
  for (int r = 0; r < 2; ++r)
#pragma unroll
    for (int i = 0; i < 13; ++i) acc[r][i] = f32x4{0.f, 0.f, 0.f, 0.f};

  stage1(0); stage1(1); stage1(2);
  const int htile = 2 + (wave < 5 ? wave : 5);
  bf16x8 wa[7], wb[7], xa[3], xb3[3];
  auto p1_read0 = [&](int s, bf16x8* xf) {
    const char* sl = lds + (s & 3) * SLOT1;
    const char* xb = sl + SW1 + SWQ + (16 + 32 * wm + l15) * 64 + ((g ^ (l15 >> 2)) * 16);
    const char* wbase = sl + (8 * g + q) * 576 + 8 * p;
    const int dsw = wn ? -32 * (g & 1) : 32 * (g & 1);
    const char* wE = wbase + wn * 288 + dsw;
    const char* wO = wbase + wn * 288 - dsw;
#define WFRAG(nt) tr_frag(((nt) & 1 ? wO : wE) + 32 * (nt), ((nt) & 1 ? wO : wE) + 32 * (nt) + 4 * 576)
    xf[0] = *(const bf16x8*)(xb);
    xf[1] = *(const bf16x8*)(xb + 16 * 64);
#pragma unroll
    for (int i = 0; i < 7; ++i) wa[i] = WFRAG(i);
    xf[2] = *(const bf16x8*)(xb - (16 + 32 * wm) * 64);
  };
  auto p1_read1 = [&](int s) {
    const char* sl = lds + (s & 3) * SLOT1;
    const char* wbase = sl + (8 * g + q) * 576 + 8 * p;
    const int dsw = wn ? -32 * (g & 1) : 32 * (g & 1);
    const char* wE = wbase + wn * 288 + dsw;
    const char* wO = wbase + wn * 288 - dsw;
    const char* qbase = sl + SW1 + (8 * g + q) * 256 + 8 * p;
    const int qsw = (q | ((g & 1) << 2));
#define QFRAG(nt) tr_frag(qbase + 32 * ((nt) ^ qsw), qbase + 32 * ((nt) ^ qsw) + 4 * 256)
    wb[0] = WFRAG(7); wb[1] = WFRAG(8);
#pragma unroll
    for (int i = 0; i < 4; ++i) wb[2 + i] = QFRAG(wn * 4 + i);
    wb[6] = QFRAG(htile);
  };
  auto p1_mma0 = [&](const bf16x8* xf) {
#pragma unroll
    for (int i = 0; i < 7; ++i) {
      acc[0][i] = __builtin_amdgcn_mfma_f32_16x16x32_bf16(wa[i], xf[0], acc[0][i], 0, 0, 0);
      acc[1][i] = __builtin_amdgcn_mfma_f32_16x16x32_bf16(wa[i], xf[1], acc[1][i], 0, 0, 0);
    }
  };
  auto p1_mma1 = [&](const bf16x8* xf) {
#pragma unroll
    for (int i = 0; i < 6; ++i) {
      acc[0][7 + i] = __builtin_amdgcn_mfma_f32_16x16x32_bf16(wb[i], xf[0], acc[0][7 + i], 0, 0, 0);
      acc[1][7 + i] = __builtin_amdgcn_mfma_f32_16x16x32_bf16(wb[i], xf[1], acc[1][7 + i], 0, 0, 0);
    }
    acch = __builtin_amdgcn_mfma_f32_16x16x32_bf16(wb[6], xf[2], acch, 0, 0, 0);
  };
  auto p1_sync = [&](int s) {
    VMWAIT(10);
    BARRIER();
    stage1(s + 3 < 18 ? s + 3 : 17);
  };
  p1_sync(0);
  p1_read0(0, xa);
#pragma unroll 1
  for (int s = 0; s < 18; s += 2) {
    p1_read1(s);
    __builtin_amdgcn_sched_barrier(0);
    p1_mma0(xa);
    p1_sync(s + 1);
    p1_read0(s + 1, xb3);
    __builtin_amdgcn_sched_barrier(0);
    p1_mma1(xa);
    p1_read1(s + 1);
    __builtin_amdgcn_sched_barrier(0);
    p1_mma0(xb3);
    p1_sync(s + 2);
    p1_read0(s + 2 < 18 ? s + 2 : 17, xa);
    __builtin_amdgcn_sched_barrier(0);
    p1_mma1(xb3);
  }
  VMWAIT(0);
  BARRIER();
  {
    char* hb = lds + OFF_H; char* qb = lds + OFF_Q;
#pragma unroll
    for (int i = 0; i < 9; ++i) {
      int f = (wn * 9 + i) * 16 + 4 * g;
      f32x4 bv = *(const f32x4*)(lds + OFF_B1 + f * 4);
#pragma unroll
      for (int r = 0; r < 2; ++r) {
        int m = 32 * wm + 16 * r + l15;
        u32x2 o;
#pragma unroll
        for (int j = 0; j < 2; ++j) {
          f32x2 v = unpk(pk_bf16(acc[r][i][2 * j], acc[r][i][2 * j + 1]));
          f32x2 bb; bb[0] = bv[2 * j]; bb[1] = bv[2 * j + 1];
          v = v + bb;
          v = unpk(pk_bf16(v[0], v[1]));
          f32x2 e = v * (v * v * (-0.044715f * 2.f * 0.7978845608028654f * 1.4426950408889634f) + (-2.f * 0.7978845608028654f * 1.4426950408889634f));
          f32x2 d; d[0] = __builtin_amdgcn_rcpf(1.f + __builtin_amdgcn_exp2f(e[0])); d[1] = __builtin_amdgcn_rcpf(1.f + __builtin_amdgcn_exp2f(e[1]));
          v = v * d;
          o[j] = pk_bf16(v[0], v[1]);
        }
        *(u32x2*)(hb + m * HS + f * 2) = o;
      }
    }
#pragma unroll
    for (int i = 9; i < 13; ++i)
#pragma unroll
      for (int r = 0; r < 2; ++r) {
        int col = wn * 64 + (i - 9) * 16 + 4 * g, row = 16 + 32 * wm + 16 * r + l15;
        u32x2 o; o[0] = pk_bf16(acc[r][i][0], acc[r][i][1]); o[1] = pk_bf16(acc[r][i][2], acc[r][i][3]);
        *(u32x2*)(qb + row * QS + col * 2) = o;
      }
    if (wave < 6) {
      int col = 32 + 16 * wave + 4 * g, row = l15;
      u32x2 o; o[0] = pk_bf16(acch[0], acch[1]); o[1] = pk_bf16(acch[2], acch[3]);
      *(u32x2*)(qb + row * QS + col * 2) = o;
    }
  }
  unsigned vo[3];
#pragma unroll
  for (int i = 0; i < 3; ++i) {
    int pc = wave + 8 * i; if (pc >= 18) pc -= 6;
    int L = 64 * pc + lane, row = L / 36, c = L % 36; c ^= 2 * ((row >> 3) & 1);
    vo[i] = (unsigned)(((fs * FC + row) * DM + c * 8) * 2);
  }
  auto stage3 = [&](int s) {
    const int nh = s >= 9, ks = s - 9 * nh;
    const unsigned sb = (unsigned)(size_t)lds + (s & 7) * SLOT3;
    const char* w2s = (const char*)w2 + (size_t)ks * 32 * DM * 2 + nh * 576;
    glds16(w2s, vo[0], sb + wave * 1024);
    glds16(w2s, vo[1], sb + (wave + 8) * 1024);
    glds16(w2s, vo[2], sb + (wave < 2 ? wave + 16 : wave + 10) * 1024);
  };
  BARRIER();

  bf16x8 hf[2][9];
  {
    const char* hb = lds + OFF_H;
#pragma unroll
    for (int ks = 0; ks < 9; ++ks) {
      hf[0][ks] = *(const bf16x8*)(hb + (32 * wm + l15) * HS + ks * 64 + g * 16);
      hf[1][ks] = *(const bf16x8*)(hb + (32 * wm + 16 + l15) * HS + ks * 64 + g * 16);
    }
  }
  BARRIER();
#pragma unroll
  for (int i = 0; i < 4; ++i) stage3(i);
  {
    const char* qb = lds + OFF_Q;
    const int row = tid >> 2, hh = (tid >> 1) & 1, h2 = tid & 1;
    const unsigned* qp = (const unsigned*)(qb + (16 + row) * QS + 36 * hh);
    float qv[18];
#pragma unroll
    for (int d = 0; d < 9; ++d) { unsigned u = qp[d]; qv[2 * d] = bf2f(u & 0xffffu); qv[2 * d + 1] = bf2f(u >> 16); }
    float lg[4];
#pragma unroll
    for (int wi = 0; wi < 4; ++wi) {
      const int w = 4 * h2 + wi;
      const unsigned* kp = (const unsigned*)(qb + (16 + row - 2 * w) * QS + 80 + 36 * hh);
      float a = 0.f;
#pragma unroll
      for (int d = 0; d < 9; ++d) { unsigned u = kp[d]; a += qv[2 * d] * bf2f(u & 0xffffu); a += qv[2 * d + 1] * bf2f(u >> 16); }
      a = rbf(rbf(a) * 0.23570226039551584f);
      lg[wi] = (r0 + row - 2 * w >= 0) ? a : -1e9f;
    }
    float mx = fmaxf(fmaxf(lg[0], lg[1]), fmaxf(lg[2], lg[3]));
    mx = fmaxf(mx, __shfl_xor(mx, 1));
    float sum = 0.f;
#pragma unroll
    for (int wi = 0; wi < 4; ++wi) { lg[wi] = __expf(lg[wi] - mx); sum += lg[wi]; }
    sum += __shfl_xor(sum, 1);
    float rs = 1.f / sum;
    float ov[18];
#pragma unroll
    for (int d = 0; d < 18; ++d) ov[d] = 0.f;
#pragma unroll
    for (int wi = 0; wi < 4; ++wi) {
      const int w = 4 * h2 + wi;
      float pw = rbf(lg[wi] * rs);
      const unsigned* vp = (const unsigned*)(qb + (16 + row - 2 * w) * QS + 160 + 36 * hh);
#pragma unroll
      for (int d = 0; d < 9; ++d) { unsigned u = vp[d]; ov[2 * d] += pw * bf2f(u & 0xffffu); ov[2 * d + 1] += pw * bf2f(u >> 16); }
    }
#pragma unroll
    for (int d = 0; d < 18; ++d) ov[d] += __shfl_xor(ov[d], 1);
    unsigned* op = (unsigned*)(o_ws + (size_t)(r0 + row) * DM + fs * 36 + 18 * hh);
    if (h2 == 0) {
#pragma unroll
      for (int d = 0; d < 5; ++d) op[d] = pk_bf16(ov[2 * d], ov[2 * d + 1]);
    } else {
#pragma unroll
      for (int d = 5; d < 9; ++d) op[d] = pk_bf16(ov[2 * d], ov[2 * d + 1]);
    }
  }
  BARRIER();
#pragma unroll
  for (int i = 4; i < 8; ++i) stage3(i);

  f32x4 acc2[2][9];
  bf16x8 va[5], vb[4];
  auto p3_read0 = [&](int s) {
    const char* sl = lds + (s & 7) * SLOT3;
    const char* wbase = sl + (8 * g + q) * 576 + 8 * p + wn * 288;
    const int dsw = wn ? -32 * (g & 1) : 32 * (g & 1);
#pragma unroll
    for (int i = 0; i < 5; ++i) {
      const char* a0 = wbase + ((i & 1) ? -dsw : dsw) + 32 * i;
      va[i] = tr_frag(a0, a0 + 4 * 576);
    }
  };
  auto p3_read1 = [&](int s) {
    const char* sl = lds + (s & 7) * SLOT3;
    const char* wbase = sl + (8 * g + q) * 576 + 8 * p + wn * 288;
    const int dsw = wn ? -32 * (g & 1) : 32 * (g & 1);
#pragma unroll
    for (int i = 5; i < 9; ++i) {
      const char* a0 = wbase + ((i & 1) ? -dsw : dsw) + 32 * i;
      vb[i - 5] = tr_frag(a0, a0 + 4 * 576);
    }
  };
  auto p3_sync = [&](int s) {
    VMWAIT(18);
    BARRIER();
    stage3(s + 7 < 18 ? s + 7 : 17);
  };
  VMWAIT(21);
  BARRIER();
  p3_read0(0);
#pragma unroll 1
  for (int nh = 0; nh < 2; ++nh) {
#pragma unroll
    for (int r = 0; r < 2; ++r)
#pragma unroll
      for (int i = 0; i < 9; ++i) acc2[r][i] = f32x4{0.f, 0.f, 0.f, 0.f};
#pragma unroll
    for (int ks = 0; ks < 9; ++ks) {
      const int s = nh * 9 + ks;
      p3_read1(s);
      __builtin_amdgcn_sched_barrier(0);
#pragma unroll
      for (int i = 0; i < 5; ++i) {
        acc2[0][i] = __builtin_amdgcn_mfma_f32_16x16x32_bf16(va[i], hf[0][ks], acc2[0][i], 0, 0, 0);
        acc2[1][i] = __builtin_amdgcn_mfma_f32_16x16x32_bf16(va[i], hf[1][ks], acc2[1][i], 0, 0, 0);
      }
      p3_sync(s + 1);
      p3_read0(s + 1 < 18 ? s + 1 : 17);
      __builtin_amdgcn_sched_barrier(0);
#pragma unroll
      for (int i = 0; i < 4; ++i) {
        acc2[0][5 + i] = __builtin_amdgcn_mfma_f32_16x16x32_bf16(vb[i], hf[0][ks], acc2[0][5 + i], 0, 0, 0);
        acc2[1][5 + i] = __builtin_amdgcn_mfma_f32_16x16x32_bf16(vb[i], hf[1][ks], acc2[1][5 + i], 0, 0, 0);
      }
    }
#pragma unroll
    for (int r = 0; r < 2; ++r)
#pragma unroll
      for (int i = 0; i < 9; ++i) {
        int n = nh * 288 + (9 * wn + i) * 16 + 4 * g, m = 32 * wm + 16 * r + l15;
        u32x2 o; o[0] = pk_bf16(acc2[r][i][0], acc2[r][i][1]); o[1] = pk_bf16(acc2[r][i][2], acc2[r][i][3]);
        { const int gm = r0 + m, nq = n / 144, nc = n - nq * 144;
          *(u32x2*)(part + ((((size_t)(gm >> 5) * 4 + nq) * 16 + fs) * 32 + (gm & 31)) * 144 + nc) = o; }
      }
  }
}

constexpr int W3ROW = 288, W3SLOT = 32 * W3ROW;
__global__ __launch_bounds__(512) void k2(const unsigned short* __restrict__ x,
                                          const unsigned short* __restrict__ wo,
                                          const unsigned short* __restrict__ b2,
                                          const unsigned short* __restrict__ o_ws,
                                          const unsigned short* __restrict__ part,
                                          unsigned short* __restrict__ out) {
  extern __shared__ __attribute__((aligned(16))) char lds[];
  const int tid = threadIdx.x, lane = tid & 63, wave = __builtin_amdgcn_readfirstlane(tid >> 6);
  const int row0 = (blockIdx.x >> 2) * 32, col0 = (blockIdx.x & 3) * 144;
  const int g = lane >> 4, l15 = lane & 15, q = l15 >> 2, p = lane & 3;
  const int r = wave & 1, nh = (wave >> 1) & 1;
  unsigned so[3];
#pragma unroll
  for (int i = 0; i < 3; ++i) {
    int pc = wave + 4 * i; if (pc >= 9) pc -= 3;
    int L = 64 * pc + lane, row = L / 18, c = L % 18;
    so[i] = (unsigned)((row * DM + col0 + c * 8) * 2);
  }
  auto stage = [&](int s) {
    if (wave >= 4) return;
    const unsigned sb = (unsigned)(size_t)lds + (s % 14) * W3SLOT;
    const char* ws = (const char*)wo + (size_t)s * 32 * DM * 2;
    glds16(ws, so[0], sb + wave * 1024);
    glds16(ws, so[1], sb + (wave + 4) * 1024);
    glds16(ws, so[2], sb + (wave == 0 ? 8 : wave + 5) * 1024);
  };
#pragma unroll
  for (int s = 0; s < 14; ++s) stage(s);
  const unsigned short* orow = o_ws + (size_t)(row0 + 16 * r + l15) * DM + 8 * g;
  bf16x8 of[18];
  if (wave < 4) {
#pragma unroll
    for (int s = 0; s < 18; ++s) of[s] = *(const bf16x8*)(orow + s * 32);
  }
  float* gs = (float*)(lds + 14 * W3SLOT);
#pragma unroll
  for (int j = 0; j < 2; ++j) {
    int c = tid + 512 * j;
    if (c < 576) {
      int rr = c / 18, cc = c % 18;
      const unsigned short* pp = part + (size_t)blockIdx.x * 16 * 32 * 144 + rr * 144 + cc * 8;
      u32x4 t[16];
#pragma unroll
      for (int f = 0; f < 16; ++f) t[f] = *(const u32x4*)(pp + f * 32 * 144);
      u32x4 bb = *(const u32x4*)(b2 + col0 + cc * 8);
      float yv[8];
#pragma unroll
      for (int e = 0; e < 8; ++e) yv[e] = 0.f;
#pragma unroll
      for (int f = 0; f < 16; ++f)
#pragma unroll
        for (int e = 0; e < 4; ++e) { yv[2 * e] += bf2f(t[f][e] & 0xffffu); yv[2 * e + 1] += bf2f(t[f][e] >> 16); }
      f32x4 g0, g1;
#pragma unroll
      for (int e = 0; e < 4; ++e) {
        float y0 = rbf(rbf(yv[2 * e]) + bf2f(bb[e] & 0xffffu)), y1 = rbf(rbf(yv[2 * e + 1]) + bf2f(bb[e] >> 16));
        float a0 = rbf(y0 / (1.f + __expf(-y0))), a1 = rbf(y1 / (1.f + __expf(-y1)));
        if (e < 2) { g0[2 * e] = a0; g0[2 * e + 1] = a1; } else { g1[2 * e - 4] = a0; g1[2 * e - 3] = a1; }
      }
      *(f32x4*)(gs + rr * 148 + cc * 8) = g0; *(f32x4*)(gs + rr * 148 + cc * 8 + 4) = g1;
    }
  }
  f32x4 acc[5];
#pragma unroll
  for (int i = 0; i < 5; ++i) acc[i] = f32x4{0.f, 0.f, 0.f, 0.f};
  VMWAIT(0);
  BARRIER();
  if (wave >= 4) { BARRIER(); BARRIER(); return; }
#pragma unroll
  for (int s = 0; s < 18; ++s) {
    if (s == 14) { VMWAIT(0); BARRIER(); }
    const char* sl = lds + (s % 14) * W3SLOT;
    const char* wrow0 = sl + (8 * g + q) * W3ROW + 8 * p;
#pragma unroll
    for (int i = 0; i < 5; ++i) {
      if (i < 4 || nh == 0) {
        const char* a0 = wrow0 + 32 * (nh * 5 + i);
        bf16x8 wf = tr_frag(a0, a0 + 4 * W3ROW);
        acc[i] = __builtin_amdgcn_mfma_f32_16x16x32_bf16(wf, of[s], acc[i], 0, 0, 0);
      }
    }
    if (s == 3) { BARRIER(); stage(14); stage(15); stage(16); stage(17); }
  }
#pragma unroll
  for (int i = 0; i < 5; ++i) {
    if (i < 4 || nh == 0) {
      int n = col0 + (nh * 5 + i) * 16 + 4 * g, m = row0 + 16 * r + l15;
      u32x2 xx = *(const u32x2*)(x + (size_t)m * DM + n);
      f32x4 gg = *(const f32x4*)(gs + (16 * r + l15) * 148 + (nh * 5 + i) * 16 + 4 * g);
      float xv[4] = {bf2f(xx[0] & 0xffffu), bf2f(xx[0] >> 16), bf2f(xx[1] & 0xffffu), bf2f(xx[1] >> 16)};
      unsigned ov[4];
#pragma unroll
      for (int j = 0; j < 4; ++j) ov[j] = f2bf(xv[j] + rbf(rbf(acc[i][j]) * gg[j]));
      u32x2 o; o[0] = ov[0] | (ov[1] << 16); o[1] = ov[2] | (ov[3] << 16);
      *(u32x2*)(out + (size_t)m * DM + n) = o;
    }
  }
}

extern "C" void kernel_launch(void* const* d_in, const int* in_sizes, int n_in,
                              void* d_out, int out_size, void* d_ws, size_t ws_size,
                              hipStream_t stream) {
  const unsigned short* x = (const unsigned short*)d_in[0];
  const unsigned short* wqkv = (const unsigned short*)d_in[1];
  const unsigned short* wo = (const unsigned short*)d_in[2];
  const unsigned short* w1 = (const unsigned short*)d_in[4];
  const unsigned short* b1 = (const unsigned short*)d_in[5];
  const unsigned short* w2 = (const unsigned short*)d_in[6];
  const unsigned short* b2 = (const unsigned short*)d_in[7];
  unsigned short* out = (unsigned short*)d_out;
  unsigned short* o_ws = (unsigned short*)d_ws;
  unsigned short* xn = (unsigned short*)((char*)d_ws + (200u << 20));
  unsigned short* part = (unsigned short*)((char*)d_ws + (4u << 20));
  hipFuncSetAttribute((const void*)k1, hipFuncAttributeMaxDynamicSharedMemorySize, LDS1);
  hipFuncSetAttribute((const void*)k2, hipFuncAttributeMaxDynamicSharedMemorySize, 14 * W3SLOT + 32 * 148 * 4);
  k0<<<1024, 64, 0, stream>>>(x, xn, (const unsigned*)w1, (const unsigned*)w2, (const unsigned*)wqkv, (const unsigned*)wo);
  k1<<<256, 512, LDS1, stream>>>(xn, wqkv, w1, b1, w2, o_ws, part);
  k2<<<256, 512, 14 * W3SLOT + 32 * 148 * 4, stream>>>(x, wo, b2, o_ws, part, out);
}
```
